```python
import torch

def _softmax(x, axis=-1):
    return torch.softmax(x, dim=axis)
def _gelu(x):
    return torch.nn.functional.gelu(x, approximate="tanh")
def _silu(x):
    return torch.nn.functional.silu(x)
def _sigmoid(x):
    return torch.sigmoid(x)
def _layernorm(x, axis=-1, eps=1e-5):
    m = x.mean(dim=axis, keepdim=True)
    v = x.var(dim=axis, keepdim=True, unbiased=False)
    return (x - m) / torch.sqrt(v + eps)
def _scatter_add(tgt, idx, src, axis=0):
    return tgt.clone().index_add_(axis, idx, src)
def _segment_sum(src, seg, n):
    out = torch.zeros((n,) + src.shape[1:], dtype=src.dtype)
    return out.index_add_(0, seg, src)
def _pad(x, pw):
    flat = tuple(v for pair in reversed(pw) for v in pair)
    return torch.nn.functional.pad(x, flat)

kernel_name = 'standalone__adm_resblock__diffusion'

def setup_inputs():

    _w = (lambda a: (lambda t: t.to(torch.bfloat16) if t.is_floating_point() else t)(torch.as_tensor(a)))
    B, C, HW, E = 2, 512, 64, 768
    sc = (9.0 * C) ** -0.5
    return {
        "x": _w((lambda *s: torch.randn(*s, dtype=torch.bfloat16) * 0.05)(B, C, HW, HW)),
        "t_emb": _w((lambda *s: torch.randn(*s, dtype=torch.bfloat16) * 0.2)(B, E)),
        "g1": _w(1.0 + 0.1 * (lambda *s: torch.randn(*s, dtype=torch.bfloat16) * 0.2)(C)),
        "be1": _w(0.1 * (lambda *s: torch.randn(*s, dtype=torch.bfloat16) * 0.2)(C)),
        "w1": _w((lambda *s: torch.randn(*s, dtype=torch.bfloat16) * 0.2)(C, C, 3, 3) * sc),
        "w_emb": _w((lambda *s: torch.randn(*s, dtype=torch.bfloat16) * 0.2)(E, C) * (E ** -0.5)),
        "g2": _w(1.0 + 0.1 * (lambda *s: torch.randn(*s, dtype=torch.bfloat16) * 0.2)(C)),
        "be2": _w(0.1 * (lambda *s: torch.randn(*s, dtype=torch.bfloat16) * 0.2)(C)),
        "w2": _w((lambda *s: torch.randn(*s, dtype=torch.bfloat16) * 0.2)(C, C, 3, 3) * sc),
    }

def reference(x, t_emb, g1, be1, w1, w_emb, g2, be2, w2):
    B, C, H, W = x.shape
    G, eps = 4, 1e-5
    def gn(z, gamma, beta):
        zr = z.reshape(B, G, (C // G) * H * W)
        mu = zr.mean(-1).reshape(B, G, 1)
        var = ((zr - mu) ** 2).mean(-1).reshape(B, G, 1)
        zn = ((zr - mu) / torch.sqrt(var + eps)).reshape(B, C, H, W)
        return zn * gamma.reshape(1, C, 1, 1) + beta.reshape(1, C, 1, 1)
    def conv3(z, w):
        zp = _pad(z, ((0, 0), (0, 0), (1, 1), (1, 1)))
        taps = [torch.einsum("bchw,oc->bohw", zp[:, :, i:i+H, j:j+W], w[:, :, i, j]) for i in range(3) for j in range(3)]
        out = taps[0]
        for t in taps[1:]:
            out = out + t
        return out
    h = conv3(_silu(gn(x, g1, be1)), w1)
    emb = _silu(t_emb) @ w_emb
    h = h + emb.reshape(B, C, 1, 1)
    h = conv3(_silu(gn(h, g2, be2)), w2)
    return x + h
```

```cpp
#include <hip/hip_runtime.h>
#include <hip/hip_bf16.h>
#include <stdint.h>
#include <utility>

#define DI __device__ __forceinline__
typedef __bf16 bf16x8 __attribute__((ext_vector_type(8)));
typedef __bf16 bf16x2 __attribute__((ext_vector_type(2)));
typedef float f32x4 __attribute__((ext_vector_type(4)));
typedef float f32x2 __attribute__((ext_vector_type(2)));
typedef unsigned u32x4 __attribute__((ext_vector_type(4)));
typedef unsigned u32x2 __attribute__((ext_vector_type(2)));
typedef unsigned short u16;
#define GAS __attribute__((address_space(1)))
typedef GAS unsigned long long gu64;

constexpr int NCH = 512, HW = 4096, NEMB = 768;
constexpr float GN_EPS = 1e-5f;

constexpr size_t WT_BYTES = (size_t)512 * 4608 * 2;
constexpr size_t OFF_WT1 = 0, OFF_WT2 = WT_BYTES;
constexpr size_t ACT_BYTES = (size_t)2 * 512 * 4096 * 2;
constexpr size_t OFF_A1 = 2 * WT_BYTES, OFF_A2 = OFF_A1 + ACT_BYTES;
constexpr size_t OFF_STAT1 = OFF_A2 + ACT_BYTES;
constexpr size_t OFF_EMB = OFF_STAT1 + 4096;
constexpr size_t OFF_GRAN = OFF_EMB + 4096;

DI float bf_lo(unsigned v) { return __uint_as_float(v << 16); }
DI float bf_hi(unsigned v) { return __uint_as_float(v & 0xffff0000u); }
DI unsigned pack_bf16(float lo, float hi) {
  f32x2 v = {lo, hi};
  bf16x2 b = __builtin_convertvector(v, bf16x2);
  return __builtin_bit_cast(unsigned, b);
}
DI float silu_f(float y) { return y * __builtin_amdgcn_rcpf(1.f + __builtin_amdgcn_exp2f(y * -1.44269504088896341f)); }
DI float wave_sum(float v) {
#pragma unroll
  for (int m = 32; m >= 1; m >>= 1) v += __shfl_xor(v, m);
  return v;
}
DI int cperm(int kg) { return ((kg & 1) << 1) | (kg >> 1); }

__global__ __launch_bounds__(256) void k0_prep(const u16* __restrict__ x, const u16* __restrict__ temb, const u16* __restrict__ w1,
                                               const u16* __restrict__ wemb, const u16* __restrict__ w2, char* __restrict__ ws) {
  __shared__ __attribute__((aligned(16))) float sm[2 * 768 + 32 * 128];
  const int bid = blockIdx.x, tid = threadIdx.x, lane = tid & 63, wid = tid >> 6;
  if (bid < 256) {
    const u32x4* src = (const u32x4*)(x + (size_t)bid * 16384);
    u32x4 v[8];
#pragma unroll
    for (int i = 0; i < 8; ++i) v[i] = src[i * 256 + tid];
    float s = 0.f;
#pragma unroll
    for (int i = 0; i < 8; ++i)
#pragma unroll
      for (int k = 0; k < 4; ++k) s += bf_lo(v[i][k]) + bf_hi(v[i][k]);
    s = wave_sum(s);
    if (lane == 0) sm[wid] = s;
    __syncthreads();
    const float tot = sm[0] + sm[1] + sm[2] + sm[3];
    const float mean = tot * (1.f / 16384.f);
    float q = 0.f;
#pragma unroll
    for (int i = 0; i < 8; ++i)
#pragma unroll
      for (int k = 0; k < 4; ++k) {
        const float a = bf_lo(v[i][k]) - mean, b = bf_hi(v[i][k]) - mean;
        q += a * a + b * b;
      }
    q = wave_sum(q);
    if (lane == 0) sm[4 + wid] = q;
    __syncthreads();
    if (tid == 0) ((f32x2*)(ws + OFF_STAT1))[bid] = f32x2{tot, sm[4] + sm[5] + sm[6] + sm[7]};
  } else if (bid < 512) {
    const int gw = (bid - 256) * 4 + wid;
    const int conv = gw >> 9, of16 = (gw >> 4) & 31, c = gw & 15;
    const int nt = of16 >> 2, of = of16 & 3, i = lane & 15, kg = lane >> 4;
    const int ocl = conv == 0 ? (32 * (of >> 1) + 8 * (i >> 2) + 4 * (of & 1) + (i & 3)) : (16 * of + i);
    const int oc = nt * 64 + ocl, ic0 = 32 * c + 8 * cperm(kg);
    const u32x4* src = (const u32x4*)((conv == 0 ? w1 : w2) + ((size_t)oc * 512 + ic0) * 9);
    unsigned d[36];
#pragma unroll
    for (int k = 0; k < 9; ++k) {
      const u32x4 t = src[k];
      d[4 * k] = t[0]; d[4 * k + 1] = t[1]; d[4 * k + 2] = t[2]; d[4 * k + 3] = t[3];
    }
    char* dst = ws + (conv == 0 ? OFF_WT1 : OFF_WT2) + (size_t)nt * 589824 + (size_t)c * 36864 + of * 1024 + lane * 16;
#pragma unroll
    for (int t = 0; t < 9; ++t) {
      const int dy = t / 3, dx = t % 3;
      u32x4 o;
#pragma unroll
      for (int k = 0; k < 4; ++k) {
        const int e0 = 9 * (2 * k) + t, e1 = 9 * (2 * k + 1) + t;
        const unsigned sel = ((e0 & 1) ? 0x0302u : 0x0100u) | (((e1 & 1) ? 0x0706u : 0x0504u) << 16);
        o[k] = __builtin_amdgcn_perm(d[e1 >> 1], d[e0 >> 1], sel);
      }
      *(u32x4*)(dst + dx * 12288 + dy * 4096) = o;
    }
  } else {
    const int e = bid - 512;
    if (e == 0) ((u32x4*)(ws + OFF_GRAN))[tid] = u32x4{0u, 0u, 0u, 0u};
    const int rg = tid >> 3, oct = tid & 7;
    u32x4 wv[24];
#pragma unroll
    for (int i = 0; i < 24; ++i) wv[i] = *(const u32x4*)(wemb + (size_t)(rg + 32 * i) * 512 + 64 * e + 8 * oct);
    for (int k = tid; k < 2 * NEMB; k += 256) sm[k] = silu_f(__uint_as_float((unsigned)temb[k] << 16));
    __syncthreads();
    float a0[8], a1[8];
#pragma unroll
    for (int j = 0; j < 8; ++j) a0[j] = a1[j] = 0.f;
#pragma unroll
    for (int i = 0; i < 24; ++i) {
      const float t0 = sm[rg + 32 * i], t1 = sm[NEMB + rg + 32 * i];
#pragma unroll
      for (int k = 0; k < 4; ++k) {
        const float lo = bf_lo(wv[i][k]), hi = bf_hi(wv[i][k]);
        a0[2 * k] += t0 * lo; a0[2 * k + 1] += t0 * hi;
        a1[2 * k] += t1 * lo; a1[2 * k + 1] += t1 * hi;
      }
    }
    float* red = sm + 2 * NEMB;
#pragma unroll
    for (int j = 0; j < 8; ++j) { red[rg * 128 + oct * 8 + j] = a0[j]; red[rg * 128 + 64 + oct * 8 + j] = a1[j]; }
    __syncthreads();
    if (tid < 128) {
      float s = 0.f;
#pragma unroll
      for (int r = 0; r < 32; ++r) s += red[r * 128 + tid];
      ((float*)(ws + OFF_EMB))[(tid >> 6) * 512 + 64 * e + (tid & 63)] = s;
    }
  }
}

DI void gn_combine(float s, float q, int lane, float n, float& mean, float& rstd) {
  const bool on = lane < 32;
  const float tot = wave_sum(on ? s : 0.f);
  mean = tot / (32.f * n);
  const float d = s / n - mean;
  const float m2 = wave_sum(on ? (q + n * d * d) : 0.f);
  rstd = 1.f / sqrtf(m2 / (32.f * n) + GN_EPS);
}

__global__ __launch_bounds__(256) void k1_norm(const u16* __restrict__ x, const u16* __restrict__ gam, const u16* __restrict__ bet,
                                               char* __restrict__ ws) {
  const int bid = blockIdx.x, tid = threadIdx.x, lane = tid & 63;
  const int b = bid >> 7, c = (bid >> 3) & 15, pg = bid & 7;
  const int oct = tid & 3, pxg = tid >> 2;
  const int ch0 = 32 * c + 8 * oct, px0 = pg * 512 + pxg * 8;
  u32x4 v[8];
#pragma unroll
  for (int j = 0; j < 8; ++j) v[j] = *(const u32x4*)(x + ((size_t)(b * NCH + ch0 + j)) * HW + px0);
  const f32x2 st = ((const f32x2*)(ws + OFF_STAT1))[(b * 4 + (c >> 2)) * 32 + (lane & 31)];
  const u32x4 gv = *(const u32x4*)(gam + ch0), bv = *(const u32x4*)(bet + ch0);
  float mean, rstd;
  gn_combine(st[0], st[1], lane, 16384.f, mean, rstd);
  float sa[8], sb[8];
#pragma unroll
  for (int k = 0; k < 4; ++k) {
    sa[2 * k] = rstd * bf_lo(gv[k]); sa[2 * k + 1] = rstd * bf_hi(gv[k]);
    sb[2 * k] = bf_lo(bv[k]) - mean * sa[2 * k]; sb[2 * k + 1] = bf_hi(bv[k]) - mean * sa[2 * k + 1];
  }
  char* dst = ws + OFF_A1 + ((size_t)(b * 16 + c) * HW + px0) * 64 + oct * 16;
#pragma unroll
  for (int p = 0; p < 8; ++p) {
    float y[8];
#pragma unroll
    for (int j = 0; j < 8; ++j) {
      const float xv = (p & 1) ? bf_hi(v[j][p >> 1]) : bf_lo(v[j][p >> 1]);
      y[j] = silu_f(xv * sa[j] + sb[j]);
    }
    u32x4 o = {pack_bf16(y[0], y[1]), pack_bf16(y[2], y[3]), pack_bf16(y[4], y[5]), pack_bf16(y[6], y[7])};
    *(u32x4*)(dst + p * 64) = o;
  }
}

constexpr int AROW = 34 * 64;
constexpr int ABUF = 24576, NABUF = 3;
constexpr int WSLOT = 12288, NWSLOT = 7;
constexpr int LDS_W = NABUF * ABUF, LDS_MISC = LDS_W + NWSLOT * WSLOT, LDS_TOTAL = 163840;
constexpr int NUNIT = 48;

struct LSched { int posW[NUNIT]; int posA[16]; int tot[NUNIT + 2]; };
constexpr LSched make_lsched() {
  LSched s{};
  int n = 0;
  n += 6; s.posA[0] = n; n += 3; s.posW[0] = n; n += 3; s.posW[1] = n; n += 3; s.posW[2] = n;
  n += 6; s.posA[1] = n; n += 3; s.posW[3] = n; n += 3; s.posW[4] = n; n += 3; s.posW[5] = n;
  n += 6; s.posA[2] = n; n += 3; s.posW[6] = n;
  s.tot[0] = n;
  for (int v = 0; v < NUNIT; ++v) {
    if (v + NWSLOT < NUNIT) { n += 3; s.posW[v + NWSLOT] = n; }
    if (v % 3 == 0 && v >= 3 && v / 3 + 2 < 16) { n += 6; s.posA[v / 3 + 2] = n; }
    s.tot[v + 1] = n;
  }
  return s;
}
constexpr LSched LS = make_lsched();
constexpr int allowed_for(int u, int issued) {
  int need = LS.posW[u];
  if (u % 3 == 0 && LS.posA[u / 3] > need) need = LS.posA[u / 3];
  int a = issued - need;
  return a > 63 ? 63 : a;
}

struct Loader {
  __amdgpu_buffer_rsrc_t ra, rw;
  unsigned va[6];
  unsigned vw;
  unsigned sa, sw;
  unsigned ma, mw;
};
template <int U> DI void ld_w(const Loader& L) {
  asm volatile("s_mov_b32 m0, %0\n\ts_nop 0\n\t"
               "buffer_load_dwordx4 %1, %2, %3 offen lds\n\t"
               "buffer_load_dwordx4 %1, %2, %3 offen offset:1024 lds\n\t"
               "buffer_load_dwordx4 %1, %2, %3 offen offset:2048 lds"
               :: "s"(L.mw + (unsigned)((U % NWSLOT) * WSLOT)), "v"(L.vw), "s"(L.rw), "s"(L.sw + (unsigned)(U * WSLOT)) : "memory");
}
template <int C> DI void ld_a(const Loader& L) {
#pragma unroll
  for (int k = 0; k < 6; ++k)
    asm volatile("s_mov_b32 m0, %0\n\ts_nop 0\n\tbuffer_load_dwordx4 %1, %2, %3 offen lds"
                 :: "s"(L.ma + (unsigned)((C % NABUF) * ABUF + k * 1024)), "v"(L.va[k]), "s"(L.ra), "s"(L.sa + (unsigned)(C * 262144)) : "memory");
}
template <int N> DI void wait_vm() { asm volatile("s_waitcnt vmcnt(%0)" :: "i"(N) : "memory"); }
DI void bar() { asm volatile("s_barrier" ::: "memory"); }

template <int V> DI void ld_interval(const Loader& L) {
  if constexpr (V + NWSLOT < NUNIT) ld_w<V + NWSLOT>(L);
  if constexpr (V % 3 == 0 && V >= 3 && V / 3 + 2 < 16) ld_a<V / 3 + 2>(L);
  wait_vm<allowed_for(V + 2, LS.tot[V + 1])>();
  bar();
}
template <int... Vs> DI void ld_intervals(const Loader& L, std::integer_sequence<int, Vs...>) { (ld_interval<Vs>(L), ...); }

struct Comp {
  f32x4 acc[8][2];
  bf16x8 A[10];
  bf16x8 B[2][6];
  unsigned abase[3];
  unsigned bbase;
  unsigned acur, anxt, bnxt;
};
struct MD { int r, dy, o; };
constexpr MD mdecode(int m) {
  int n = 0;
  for (int r = 0; r < 10; ++r)
    for (int dy = 0; dy < 3; ++dy) {
      if (r - dy < 0 || r - dy > 7) continue;
      for (int o = 0; o < 2; ++o) { if (n == m) return MD{r, dy, o}; ++n; }
    }
  return MD{-1, -1, -1};
}
DI bf16x8 lds_rd(const char* lds, unsigned addr, int off) { return *(const bf16x8*)(lds + addr + off); }

template <int MODE, int PAR, int M> DI void mstep(Comp& s, const char* lds) {
  constexpr MD d = mdecode(M);
  if constexpr (MODE == 1) s.acc[d.r - d.dy][d.o] = __builtin_amdgcn_mfma_f32_16x16x32_bf16(s.B[PAR][d.dy * 2 + d.o], s.A[d.r], s.acc[d.r - d.dy][d.o], 0, 0, 0);
  else                     s.acc[d.r - d.dy][d.o] = __builtin_amdgcn_mfma_f32_16x16x32_bf16(s.A[d.r], s.B[PAR][d.dy * 2 + d.o], s.acc[d.r - d.dy][d.o], 0, 0, 0);
  if constexpr (M % 3 == 0) {
    constexpr int j = M / 3;
    if constexpr (j < 6) s.A[4 + j] = lds_rd(lds, s.acur, (4 + j) * AROW);
    else if constexpr (j == 6) s.B[PAR ^ 1][0] = lds_rd(lds, s.bnxt, 0);
    else if constexpr (j == 7) s.B[PAR ^ 1][1] = lds_rd(lds, s.bnxt, 1024);
    else if constexpr (j == 8) s.A[0] = lds_rd(lds, s.anxt, 0);
    else if constexpr (j == 9) s.B[PAR ^ 1][2] = lds_rd(lds, s.bnxt, 4096);
    else if constexpr (j == 10) s.B[PAR ^ 1][3] = lds_rd(lds, s.bnxt, 4096 + 1024);
    else if constexpr (j == 11) s.B[PAR ^ 1][4] = lds_rd(lds, s.bnxt, 8192);
    else if constexpr (j == 12) s.B[PAR ^ 1][5] = lds_rd(lds, s.bnxt, 8192 + 1024);
    else s.A[j - 12] = lds_rd(lds, s.anxt, (j - 12) * AROW);
  }
  __builtin_amdgcn_sched_barrier(0);
}
template <int MODE, int PAR, int... Ms> DI void msteps(Comp& s, const char* lds, std::integer_sequence<int, Ms...>) { (mstep<MODE, PAR, Ms>(s, lds), ...); }

template <int MODE, int PAR, int DX> DI void unit(Comp& s, const char* lds, unsigned& aoff, unsigned& woff) {
  bar();
  s.acur = s.anxt;
  if constexpr (DX == 2) { aoff += ABUF; if (aoff == NABUF * ABUF) aoff = 0; }
  s.anxt = s.abase[(DX + 1) % 3] + aoff;
  woff += WSLOT; if (woff == NWSLOT * WSLOT) woff = 0;
  s.bnxt = s.bbase + woff;
  __builtin_amdgcn_sched_barrier(0);
  msteps<MODE, PAR>(s, lds, std::make_integer_sequence<int, 48>{});
  asm volatile("s_waitcnt lgkmcnt(3)" ::: "memory");
}

template <int MODE>
__global__ __launch_bounds__(512, 2) void k_conv(const char* __restrict__ act, const char* __restrict__ wt, const u16* __restrict__ gam,
                                                 const u16* __restrict__ bet, const u16* __restrict__ xres, u16* __restrict__ out,
                                                 char* __restrict__ ws) {
  __shared__ __attribute__((aligned(16))) char lds[LDS_TOTAL];
  const int tid = threadIdx.x, lane = tid & 63;
  const int wid = __builtin_amdgcn_readfirstlane(tid >> 6);
  const int bid = blockIdx.x, xcd = bid & 7, slot = bid >> 3;
  const int b = xcd >> 2, grp = xcd & 3, nt = 2 * grp + (slot & 1), mt = slot >> 1;
  const int y0 = (mt >> 1) * 8, x0 = (mt & 1) * 32;
  const unsigned ldsb = (unsigned)(size_t)lds;

  if (wid >= 4) {
    const int w = wid - 4;
    Loader L;
    L.ra = __builtin_amdgcn_make_buffer_rsrc((void*)act, (short)0, (int)ACT_BYTES, 0x00020000);
    L.rw = __builtin_amdgcn_make_buffer_rsrc((void*)wt, (short)0, (int)WT_BYTES, 0x00020000);
#pragma unroll
    for (int k = 0; k < 6; ++k) {
      const int q = (w * 6 + k) * 16 + (lane >> 2), row = q / 34, col = q - row * 34;
      const int gy = y0 - 1 + row, gx = x0 - 1 + col;
      const bool ok = q < 340 && gy >= 0 && gy < 64 && gx >= 0 && gx < 64;
      const int c16 = (lane & 3) ^ ((col >> 2) & 1);
      L.va[k] = ok ? (unsigned)((gy * 64 + gx) * 64 + c16 * 16) : 0x80000000u;
    }
    L.vw = lane * 16;
    L.sa = (unsigned)b * 16u * 262144u;
    L.sw = (unsigned)nt * 589824u + (unsigned)w * 3072u;
    L.ma = ldsb + w * 6144;
    L.mw = ldsb + LDS_W + w * 3072;
    ld_a<0>(L); ld_w<0>(L); ld_w<1>(L); ld_w<2>(L); ld_a<1>(L); ld_w<3>(L); ld_w<4>(L); ld_w<5>(L); ld_a<2>(L); ld_w<6>(L);
    wait_vm<allowed_for(0, LS.tot[0])>(); bar();
    wait_vm<allowed_for(1, LS.tot[0])>(); bar();
    ld_intervals(L, std::make_integer_sequence<int, NUNIT - 2>{});
    bar();
    return;
  }

  const int hx = wid & 1, ho = wid >> 1;
  const int i16 = lane & 15, kg = lane >> 4;
  Comp s;
#pragma unroll
  for (int dx = 0; dx < 3; ++dx) {
    const int col = 16 * hx + i16 + dx;
    s.abase[dx] = col * 64 + ((cperm(kg) ^ ((col >> 2) & 1)) << 4);
  }
  s.bbase = LDS_W + ho * 2048 + lane * 16;
#pragma unroll
  for (int r = 0; r < 8; ++r) { s.acc[r][0] = f32x4{0.f, 0.f, 0.f, 0.f}; s.acc[r][1] = f32x4{0.f, 0.f, 0.f, 0.f}; }

  [[maybe_unused]] u32x2 xr[8][2];
  if constexpr (MODE == 2) {
#pragma unroll
    for (int r = 0; r < 8; ++r)
#pragma unroll
      for (int f = 0; f < 2; ++f)
        xr[r][f] = *(const u32x2*)(xres + ((size_t)(b * NCH + nt * 64 + 32 * ho + 16 * f + i16)) * HW + (y0 + r) * 64 + x0 + 16 * hx + 4 * kg);
  }

  bar();
#pragma unroll
  for (int k = 0; k < 6; ++k) s.B[0][k] = lds_rd(lds, s.bbase, (k >> 1) * 4096 + (k & 1) * 1024);
#pragma unroll
  for (int r = 0; r < 4; ++r) s.A[r] = lds_rd(lds, s.abase[0], r * AROW);
  s.anxt = s.abase[0];
  unsigned aoff = 0, woff = 0;
  __builtin_amdgcn_sched_barrier(0);
#pragma unroll 1
  for (int cc = 0; cc < 8; ++cc) {
    unit<MODE, 0, 0>(s, lds, aoff, woff);
    unit<MODE, 1, 1>(s, lds, aoff, woff);
    unit<MODE, 0, 2>(s, lds, aoff, woff);
    unit<MODE, 1, 0>(s, lds, aoff, woff);
    unit<MODE, 0, 1>(s, lds, aoff, woff);
    unit<MODE, 1, 2>(s, lds, aoff, woff);
  }

  if constexpr (MODE == 1) {
    const int ch0 = nt * 64 + 32 * ho + 8 * kg;
    const float* embp = (const float*)(ws + OFF_EMB) + b * 512 + ch0;
    const f32x4 e0 = *(const f32x4*)embp, e1 = *(const f32x4*)(embp + 4);
    const u32x4 gv = *(const u32x4*)(gam + ch0), bv = *(const u32x4*)(bet + ch0);
    float sum = 0.f;
#pragma unroll
    for (int r = 0; r < 8; ++r) {
      s.acc[r][0] += e0; s.acc[r][1] += e1;
#pragma unroll
      for (int j = 0; j < 4; ++j) sum += s.acc[r][0][j] + s.acc[r][1][j];
    }
    float* red = (float*)(lds + LDS_MISC);
    sum = wave_sum(sum);
    if (lane == 0) red[wid] = sum;
    asm volatile("s_waitcnt lgkmcnt(0)" ::: "memory"); bar();
    const float tot = red[0] + red[1] + red[2] + red[3];
    const float bmean = tot * (1.f / 16384.f);
    float q = 0.f;
#pragma unroll
    for (int r = 0; r < 8; ++r)
#pragma unroll
      for (int f = 0; f < 2; ++f)
#pragma unroll
        for (int j = 0; j < 4; ++j) { const float d = s.acc[r][f][j] - bmean; q += d * d; }
    q = wave_sum(q);
    if (lane == 0) red[4 + wid] = q;
    asm volatile("s_waitcnt lgkmcnt(0)" ::: "memory"); bar();
    gu64* gran = (gu64*)(ws + OFF_GRAN) + xcd * 64;
    if (wid == 0) {
      const float m2 = red[4] + red[5] + red[6] + red[7];
      if (lane < 2) {
        const float val = lane == 0 ? tot : m2;
        __hip_atomic_store(gran + slot * 2 + lane, (1ull << 32) | (unsigned long long)__float_as_uint(val), __ATOMIC_RELAXED, __HIP_MEMORY_SCOPE_AGENT);
      }
      unsigned long long g;
      for (unsigned spins = 0;; ++spins) {
        g = __hip_atomic_load(gran + lane, __ATOMIC_RELAXED, __HIP_MEMORY_SCOPE_AGENT);
        if (__all((unsigned)(g >> 32) == 1u)) break;
        if (spins > (1u << 20)) break;
        __builtin_amdgcn_s_sleep(1);
      }
      const float val = __uint_as_float((unsigned)g);
      const float other = __shfl_xor(val, 1);
      const float sk = (lane & 1) ? other : val, qk = (lane & 1) ? val : other;
      const float gtot = wave_sum((lane & 1) ? 0.f : sk);
      const float mean = gtot * (1.f / (32.f * 16384.f));
      const float d = sk * (1.f / 16384.f) - mean;
      const float gm2 = wave_sum((lane & 1) ? 0.f : (qk + 16384.f * d * d));
      const float rstd = 1.f / sqrtf(gm2 * (1.f / (32.f * 16384.f)) + GN_EPS);
      if (lane == 0) { red[8] = mean; red[9] = rstd; }
    }
    asm volatile("s_waitcnt lgkmcnt(0)" ::: "memory"); bar();
    const float mean = red[8], rstd = red[9];
    float sa[8], sb[8];
#pragma unroll
    for (int k = 0; k < 4; ++k) {
      sa[2 * k] = rstd * bf_lo(gv[k]); sa[2 * k + 1] = rstd * bf_hi(gv[k]);
      sb[2 * k] = bf_lo(bv[k]) - mean * sa[2 * k]; sb[2 * k + 1] = bf_hi(bv[k]) - mean * sa[2 * k + 1];
    }
    char* dst = ws + OFF_A2 + ((size_t)(b * 16 + 2 * nt + ho) * HW + y0 * 64 + x0 + 16 * hx + i16) * 64 + kg * 16;
#pragma unroll
    for (int r = 0; r < 8; ++r) {
      float y[8];
#pragma unroll
      for (int j = 0; j < 4; ++j) {
        y[j] = silu_f(s.acc[r][0][j] * sa[j] + sb[j]);
        y[4 + j] = silu_f(s.acc[r][1][j] * sa[4 + j] + sb[4 + j]);
      }
      u32x4 o = {pack_bf16(y[0], y[1]), pack_bf16(y[2], y[3]), pack_bf16(y[4], y[5]), pack_bf16(y[6], y[7])};
      *(u32x4*)(dst + (size_t)r * 64 * 64) = o;
    }
  } else {
#pragma unroll
    for (int r = 0; r < 8; ++r)
#pragma unroll
      for (int f = 0; f < 2; ++f) {
        const f32x4 a = s.acc[r][f];
        u32x2 o;
        o[0] = pack_bf16(a[0] + bf_lo(xr[r][f][0]), a[1] + bf_hi(xr[r][f][0]));
        o[1] = pack_bf16(a[2] + bf_lo(xr[r][f][1]), a[3] + bf_hi(xr[r][f][1]));
        *(u32x2*)(out + ((size_t)(b * NCH + nt * 64 + 32 * ho + 16 * f + i16)) * HW + (y0 + r) * 64 + x0 + 16 * hx + 4 * kg) = o;
      }
  }
}

extern "C" void kernel_launch(void* const* d_in, const int* in_sizes, int n_in, void* d_out, int out_size, void* d_ws, size_t ws_size,
                              hipStream_t stream) {
  const u16* x = (const u16*)d_in[0];
  const u16* temb = (const u16*)d_in[1];
  const u16* g1 = (const u16*)d_in[2];
  const u16* be1 = (const u16*)d_in[3];
  const u16* w1 = (const u16*)d_in[4];
  const u16* wemb = (const u16*)d_in[5];
  const u16* g2 = (const u16*)d_in[6];
  const u16* be2 = (const u16*)d_in[7];
  const u16* w2 = (const u16*)d_in[8];
  char* ws = (char*)d_ws;
  k0_prep<<<520, 256, 0, stream>>>(x, temb, w1, wemb, w2, ws);
  k1_norm<<<256, 256, 0, stream>>>(x, g1, be1, ws);
  k_conv<1><<<256, 512, 0, stream>>>(ws + OFF_A1, ws + OFF_WT1, g2, be2, nullptr, nullptr, ws);
  k_conv<2><<<256, 512, 0, stream>>>(ws + OFF_A2, ws + OFF_WT2, nullptr, nullptr, x, (u16*)d_out, ws);
}
```

```cpp
#include <hip/hip_runtime.h>
#include <hip/hip_bf16.h>
#include <stdint.h>
#include <utility>

#define DI __device__ __forceinline__
typedef __bf16 bf16x8 __attribute__((ext_vector_type(8)));
typedef __bf16 bf16x2 __attribute__((ext_vector_type(2)));
typedef float f32x4 __attribute__((ext_vector_type(4)));
typedef float f32x2 __attribute__((ext_vector_type(2)));
typedef unsigned u32x4 __attribute__((ext_vector_type(4)));
typedef unsigned u32x2 __attribute__((ext_vector_type(2)));
typedef unsigned short u16;
#define GAS __attribute__((address_space(1)))
typedef GAS unsigned long long gu64;
typedef GAS unsigned gu32;
#define RLX_AGENT __ATOMIC_RELAXED, __HIP_MEMORY_SCOPE_AGENT

constexpr int NCH = 512, HW = 4096, NEMB = 768;
constexpr float GN_EPS = 1e-5f;
constexpr unsigned SPIN_CAP = 1u << 20;

constexpr size_t U_BYTES = (size_t)8 * 16 * 49152;
constexpr size_t V_BYTES = (size_t)2 * 16 * 4 * 131072;
constexpr size_t OFF_U1 = 0, OFF_U2 = U_BYTES;
constexpr size_t OFF_V1 = 2 * U_BYTES, OFF_V2 = OFF_V1 + V_BYTES;
constexpr size_t OFF_EPOCH = OFF_V2 + V_BYTES;
constexpr size_t OFF_GRAN = OFF_EPOCH + 256;
constexpr size_t OFF_LINES = OFF_GRAN + 8192;
constexpr int W_HALO = 0, W_V1 = 4, W_V2 = 12, W_U1 = 20, W_U2 = 24;
constexpr size_t OFF_EMBP = OFF_LINES + 256 * 128;
constexpr size_t OFF_HALO = OFF_EMBP + 8 * 32 * 512;
constexpr int HALO_STRIDE = 18432;
constexpr size_t OFF_DIAG = OFF_HALO + 256 * HALO_STRIDE;
#define STAMP(i)
#define STAMPR(i)

DI float bf_lo(unsigned v) { return __uint_as_float(v << 16); }
DI float bf_hi(unsigned v) { return __uint_as_float(v & 0xffff0000u); }
DI unsigned pack_bf16(float lo, float hi) {
  f32x2 v = {lo, hi};
  bf16x2 b = __builtin_convertvector(v, bf16x2);
  return __builtin_bit_cast(unsigned, b);
}
DI float silu_f(float y) { return y * __builtin_amdgcn_rcpf(1.f + __builtin_amdgcn_exp2f(y * -1.44269504088896341f)); }
DI f32x2 pk_sub(f32x2 a, f32x2 b) { return __builtin_elementwise_fma(b, f32x2{-1.f, -1.f}, a); }
DI f32x2 lo2(const f32x4& v) { return __builtin_shufflevector(v, v, 0, 1); }
DI f32x2 hi2(const f32x4& v) { return __builtin_shufflevector(v, v, 2, 3); }
DI f32x4 cat2(f32x2 a, f32x2 b) { return __builtin_shufflevector(a, b, 0, 1, 2, 3); }
DI f32x4 sub4(const f32x4& a, const f32x4& b) { return cat2(pk_sub(lo2(a), lo2(b)), pk_sub(hi2(a), hi2(b))); }
DI f32x2 silu_aff2(f32x2 h, f32x2 sa, f32x2 sb, f32x2 sc, f32x2 sd) {
  const f32x2 y = __builtin_elementwise_fma(h, sa, sb), t = __builtin_elementwise_fma(h, sc, sd);
  const f32x2 d = f32x2{__builtin_amdgcn_exp2f(t[0]), __builtin_amdgcn_exp2f(t[1])} + 1.f;
  return y * f32x2{__builtin_amdgcn_rcpf(d[0]), __builtin_amdgcn_rcpf(d[1])};
}
template <int CTRL, int RM = 0xf> DI float dpp0(float v) {
  return __builtin_bit_cast(float, __builtin_amdgcn_update_dpp(0, __builtin_bit_cast(int, v), CTRL, RM, 0xf, true));
}
DI float dpp_prev(float v) { return dpp0<0x111>(v); }
DI float dpp_next(float v) { return dpp0<0x101>(v); }
DI float wave_sum(float v) {
  v += dpp0<0x111>(v); v += dpp0<0x112>(v); v += dpp0<0x114>(v); v += dpp0<0x118>(v);
  v += dpp0<0x142, 0xa>(v);
  v += dpp0<0x143, 0xc>(v);
  return __builtin_bit_cast(float, __builtin_amdgcn_readlane(__builtin_bit_cast(int, v), 63));
}
DI int vpos(int o, int xt) { return (((o & 1) << 1) | (o >> 1)) ^ ((xt >> 2) & 1); }
DI void bar() { asm volatile("s_barrier" ::: "memory"); }
DI void lds_bar() { asm volatile("s_waitcnt lgkmcnt(0)\n\ts_barrier" ::: "memory"); }
template <int N> DI void wait_vm() { asm volatile("s_waitcnt vmcnt(%0)" :: "i"(N) : "memory"); }

DI void gn_combine(float s, float q, bool on, float n, float& mean, float& rstd) {
  const float tot = wave_sum(on ? s : 0.f);
  mean = tot * (1.f / (32.f * n));
  const float d = s * (1.f / n) - mean;
  const float m2 = wave_sum(on ? (q + n * d * d) : 0.f);
  rstd = __builtin_amdgcn_rsqf(m2 * (1.f / (32.f * n)) + GN_EPS);
}

constexpr int VSLOT = 20480, NVSLOT = 4;
constexpr int USLOT = 24576, NUSLOT = 3;
constexpr int LDS_U = NVSLOT * VSLOT, LDS_MISC = LDS_U + NUSLOT * USLOT, LDS_TOTAL = 163840;
constexpr int NUNIT = 32;
constexpr int M_EMB = LDS_MISC, M_GAM = LDS_MISC + 256, M_BET = LDS_MISC + 512, M_RED = LDS_MISC + 768, M_ZERO = LDS_MISC + 896, M_RAW = LDS_MISC + 1024, M_COL = LDS_MISC + 3584;
constexpr int X_HAND = 65536;

struct Tile { int bid, b, nt, ty, tx, y0; };
DI int tile_bid(int b, int ty, int tx, int nt) { return (((ty & 3) * 8 + tx * 4 + (nt & 3)) << 3) | (b * 4 + (ty >> 2) * 2 + (nt >> 2)); }
DI int chunk_of(int j, int nt) { return j < 2 ? 2 * nt + j : (j - 2 < 2 * nt ? j - 2 : j); }

struct Loader {
  __amdgpu_buffer_rsrc_t rv, ru;
  unsigned vfirst, vmid, vlast;
  unsigned sv, su;
  unsigned mv, mu;
  int nt;
};
DI void dma1(unsigned m0, unsigned voff, __amdgpu_buffer_rsrc_t r, unsigned soff) {
  asm volatile("s_mov_b32 m0, %0\n\ts_nop 0\n\tbuffer_load_dwordx4 %1, %2, %3 offen lds" :: "s"(m0), "v"(voff), "s"(r), "s"(soff) : "memory");
}
DI void dma1_dev(unsigned m0, unsigned voff, __amdgpu_buffer_rsrc_t r, unsigned soff) {
  asm volatile("s_mov_b32 m0, %0\n\ts_nop 0\n\tbuffer_load_dwordx4 %1, %2, %3 offen sc1 lds" :: "s"(m0), "v"(voff), "s"(r), "s"(soff) : "memory");
}
DI void ld_u(const Loader& L, int k) {
  const unsigned mu = L.mu + (unsigned)(k % NUSLOT) * USLOT, su = L.su + (unsigned)(chunk_of(k >> 1, L.nt) * 2 + (k & 1)) * USLOT;
  asm volatile("s_mov_b32 m0, %0\n\ts_nop 0\n\t"
               "buffer_load_dwordx4 %1, %2, %3 offen lds\n\t"
               "buffer_load_dwordx4 %1, %2, %3 offen offset:1024 lds\n\t"
               "buffer_load_dwordx4 %1, %2, %3 offen offset:2048 lds\n\t"
               "buffer_load_dwordx4 %1, %2, %3 offen offset:3072 lds"
               :: "s"(mu), "v"(L.vmid), "s"(L.ru), "s"(su) : "memory");
  asm volatile("s_mov_b32 m0, %0\n\ts_nop 0\n\t"
               "buffer_load_dwordx4 %1, %2, %3 offen lds\n\t"
               "buffer_load_dwordx4 %1, %2, %3 offen offset:1024 lds"
               :: "s"(mu + 4096), "v"(L.vmid), "s"(L.ru), "s"(su + 4096) : "memory");
}
DI void ld_v(const Loader& L, int k) {
  const unsigned mv = L.mv + (unsigned)(k & 3) * VSLOT, sv = L.sv + (unsigned)(chunk_of(k >> 1, L.nt) * 2 + (k & 1)) * 262144u;
  dma1_dev(mv, L.vfirst, L.rv, sv);
  dma1_dev(mv + 1024, L.vmid, L.rv, sv + 2048);
  dma1_dev(mv + 2048, L.vmid, L.rv, sv + 4096);
  dma1_dev(mv + 3072, L.vmid, L.rv, sv + 6144);
  dma1_dev(mv + 4096, L.vlast, L.rv, sv + 8192);
}

struct Comp {
  f32x4 acc[2][8][2];
  bf16x8 A[10];
  bf16x8 B[2][6];
  unsigned abase, bbase;
  unsigned acur, anxt, bnxt;
};
struct MD { int r, dy, o; };
constexpr MD mdecode(int m) {
  int n = 0;
  for (int r = 0; r < 10; ++r)
    for (int dy = 0; dy < 3; ++dy) {
      if (r - dy < 0 || r - dy > 7) continue;
      for (int o = 0; o < 2; ++o) { if (n == m) return MD{r, dy, o}; ++n; }
    }
  return MD{-1, -1, -1};
}
template <int OFF> DI void lds_rd(bf16x8& dst, unsigned addr) { asm volatile("ds_read_b128 %0, %1 offset:%2" : "=v"(dst) : "v"(addr), "i"(OFF)); }
template <int N> DI void wait_lgkm() { asm volatile("s_waitcnt lgkmcnt(%0)" :: "i"(N) : "memory"); }

constexpr int first_wait(int m) {
  switch (m) { case 2: return 3; case 6: return 3; case 12: return 4; case 18: return 5; case 24: return 6; case 30: return 7;
               case 36: return 8; case 42: return 9; case 46: return 10; default: return -1; }
}
template <int MODE, int E, int M> DI void mstep(Comp& s) {
  constexpr MD d = mdecode(M);
  if constexpr (first_wait(M) >= 0) wait_lgkm<first_wait(M)>();
  if constexpr (MODE == 1) asm volatile("v_mfma_f32_16x16x32_bf16 %0, %1, %2, %0" : "+v"(s.acc[E][d.r - d.dy][d.o]) : "v"(s.B[E][d.dy * 2 + d.o]), "v"(s.A[d.r]));
  else                     asm volatile("v_mfma_f32_16x16x32_bf16 %0, %1, %2, %0" : "+v"(s.acc[E][d.r - d.dy][d.o]) : "v"(s.A[d.r]), "v"(s.B[E][d.dy * 2 + d.o]));
  if constexpr (M % 3 == 0) {
    constexpr int j = M / 3;
    if constexpr (j < 6) lds_rd<(4 + j) * 1024>(s.A[4 + j], s.acur);
    else if constexpr (j == 6) lds_rd<0>(s.B[E ^ 1][0], s.bnxt);
    else if constexpr (j == 7) lds_rd<1024>(s.B[E ^ 1][1], s.bnxt);
    else if constexpr (j == 8) lds_rd<0>(s.A[0], s.anxt);
    else if constexpr (j == 9) lds_rd<4096>(s.B[E ^ 1][2], s.bnxt);
    else if constexpr (j == 10) lds_rd<4096 + 1024>(s.B[E ^ 1][3], s.bnxt);
    else if constexpr (j == 11) lds_rd<8192>(s.B[E ^ 1][4], s.bnxt);
    else if constexpr (j == 12) lds_rd<8192 + 1024>(s.B[E ^ 1][5], s.bnxt);
    else lds_rd<(j - 12) * 1024>(s.A[j - 12], s.anxt);
  }
}
template <int MODE, int E, int... Ms> DI void msteps(Comp& s, std::integer_sequence<int, Ms...>) { (mstep<MODE, E, Ms>(s), ...); }

template <int MODE, int E> DI void unit(Comp& s, unsigned& voff, unsigned& uoff) {
  bar();
  s.acur = s.anxt;
  voff = (voff + VSLOT) == NVSLOT * VSLOT ? 0u : voff + VSLOT;
  s.anxt = s.abase + voff;
  uoff = (uoff + USLOT) == NUSLOT * USLOT ? 0u : uoff + USLOT;
  s.bnxt = s.bbase + uoff;
  msteps<MODE, E>(s, std::make_integer_sequence<int, 48>{});
  wait_lgkm<3>();
}

struct Affine { f32x2 sa[4], sb[4], sc[4], sd[4]; };
DI void row_transform(const f32x4 (&ye)[2], const f32x4 (&yo)[2], const f32x4 (&hl)[2], const f32x4 (&hr)[2], const Affine& af, u32x4 (&o)[4]) {
#pragma unroll
  for (int q = 0; q < 4; ++q) {
    const f32x2 he = (q & 1) ? hi2(ye[q >> 1]) : lo2(ye[q >> 1]), ho2 = (q & 1) ? hi2(yo[q >> 1]) : lo2(yo[q >> 1]);
    const f32x2 d1 = silu_aff2(he, af.sa[q], af.sb[q], af.sc[q], af.sd[q]);
    const f32x2 d2 = silu_aff2(ho2, af.sa[q], af.sb[q], af.sc[q], af.sd[q]);
    const f32x2 d2l = pk_sub(d2, (q & 1) ? hi2(hl[q >> 1]) : lo2(hl[q >> 1])), d1r = pk_sub(d1, (q & 1) ? hi2(hr[q >> 1]) : lo2(hr[q >> 1]));
    const f32x2 t1 = d1 + d2, t2 = pk_sub(d2, d1);
    o[0][q] = pack_bf16(dpp_prev(d2[0]) - d2l[0], dpp_prev(d2[1]) - d2l[1]);
    o[1][q] = pack_bf16(t1[0], t1[1]);
    o[2][q] = pack_bf16(t2[0], t2[1]);
    o[3][q] = pack_bf16(d1r[0] - dpp_next(d1[0]), d1r[1] - dpp_next(d1[1]));
  }
}
DI void halo_ops(const char* lds, const float* c, int tx, int i16, int kg, f32x4 (&hl)[2], f32x4 (&hr)[2]) {
  const float* z = (const float*)(lds + M_ZERO);
  const float* pl = (tx == 1 && i16 == 0) ? c + 8 * kg : z;
  const float* pr = (tx == 0 && i16 == 15) ? c + 8 * kg : z;
  hl[0] = *(const f32x4*)pl; hl[1] = *(const f32x4*)(pl + 4);
  hr[0] = *(const f32x4*)pr; hr[1] = *(const f32x4*)(pr + 4);
}

template <int P> DI void exchange(const Comp& s, char* lds, int wid, int lane, f32x4 (&ye)[4][2], f32x4 (&yo)[4][2]) {
  char* xb = lds + wid * 16384 + lane * 16;
  const char* rb = lds + (wid ^ 1) * 16384 + lane * 16;
#pragma unroll
  for (int r = 0; r < 4; ++r)
#pragma unroll
    for (int o = 0; o < 2; ++o) {
      if constexpr (P == 0) {
        *(f32x4*)(xb + (r * 4 + o * 2) * 1024) = s.acc[0][4 + r][o] + s.acc[1][4 + r][o];
        *(f32x4*)(xb + (r * 4 + o * 2 + 1) * 1024) = s.acc[1][4 + r][o];
      } else {
        *(f32x4*)(xb + (r * 4 + o * 2) * 1024) = s.acc[0][r][o];
        *(f32x4*)(xb + (r * 4 + o * 2 + 1) * 1024) = s.acc[0][r][o] + s.acc[1][r][o];
      }
    }
  lds_bar();
  f32x4 r0[4][2], r1[4][2];
#pragma unroll
  for (int r = 0; r < 4; ++r)
#pragma unroll
    for (int o = 0; o < 2; ++o) { r0[r][o] = *(const f32x4*)(rb + (r * 4 + o * 2) * 1024); r1[r][o] = *(const f32x4*)(rb + (r * 4 + o * 2 + 1) * 1024); }
#pragma unroll
  for (int r = 0; r < 4; ++r)
#pragma unroll
    for (int o = 0; o < 2; ++o) {
      if constexpr (P == 0) { ye[r][o] = s.acc[0][r][o] + s.acc[1][r][o] + r0[r][o]; yo[r][o] = sub4(s.acc[1][r][o], r1[r][o]); }
      else                  { ye[r][o] = r0[r][o] + s.acc[0][4 + r][o]; yo[r][o] = sub4(r1[r][o], s.acc[0][4 + r][o] + s.acc[1][4 + r][o]); }
    }
}

DI void load_affine(Affine& af, const char* lds, int ho, int kg) {
  const float* red = (const float*)(lds + M_RED);
  const float mean = red[16], rstd = red[17];
  const f32x4 g0 = *(const f32x4*)(lds + M_GAM + (32 * ho + 8 * kg) * 4), g1 = *(const f32x4*)(lds + M_GAM + (32 * ho + 8 * kg) * 4 + 16);
  const f32x4 b0 = *(const f32x4*)(lds + M_BET + (32 * ho + 8 * kg) * 4), b1 = *(const f32x4*)(lds + M_BET + (32 * ho + 8 * kg) * 4 + 16);
#pragma unroll
  for (int q = 0; q < 4; ++q) {
    const f32x2 g = q == 0 ? lo2(g0) : q == 1 ? hi2(g0) : q == 2 ? lo2(g1) : hi2(g1);
    const f32x2 bb = q == 0 ? lo2(b0) : q == 1 ? hi2(b0) : q == 2 ? lo2(b1) : hi2(b1);
    af.sa[q] = g * rstd; af.sb[q] = bb - af.sa[q] * mean;
    af.sc[q] = af.sa[q] * -1.44269504088896341f; af.sd[q] = af.sb[q] * -1.44269504088896341f;
  }
}
DI void set_tag(char* ws, int bid, int word, unsigned ep, int lane) {
  if (lane == 0) __hip_atomic_store((gu32*)(ws + OFF_LINES) + bid * 32 + word, ep, RLX_AGENT);
}
template <int NQ> DI void wait_tags(char* ws, int who, int w0, bool need, unsigned ep) {
  const __amdgpu_buffer_rsrc_t rl = __builtin_amdgcn_make_buffer_rsrc((void*)(ws + OFF_LINES), (short)0, 256 * 128, 0x00020000);
  for (unsigned spins = 0;; ++spins) {
    bool ok = true;
#pragma unroll
    for (int q = 0; q < NQ; ++q) {
      const u32x4 v = __builtin_amdgcn_raw_buffer_load_b128(rl, who * 128 + w0 * 4 + q * 16, 0, 16);
      ok = ok && v[0] == ep && v[1] == ep && v[2] == ep && v[3] == ep;
    }
    if (__all(!need || ok)) break;
    if (spins > SPIN_CAP) break;
    __builtin_amdgcn_s_sleep(2);
    asm volatile("" ::: "memory");
  }
  asm volatile("" ::: "memory");
}
DI void group_stats(char* ws, int conv, const Tile& T, float tot, float m2, unsigned ep, float* red, int lane) {
  gu64* gran = (gu64*)(ws + OFF_GRAN) + (conv * 8 + T.b * 4 + (T.nt >> 1)) * 64;
  if (lane < 2) {
    const float val = lane == 0 ? tot : m2;
    __hip_atomic_store(gran + (T.ty * 4 + T.tx * 2 + (T.nt & 1)) * 2 + lane, ((unsigned long long)ep << 32) | (unsigned long long)__float_as_uint(val), RLX_AGENT);
  }
}
DI void group_stats_wait(char* ws, int conv, const Tile& T, unsigned ep, float* red, int lane) {
  gu64* gran = (gu64*)(ws + OFF_GRAN) + (conv * 8 + T.b * 4 + (T.nt >> 1)) * 64;
  unsigned long long g;
  for (unsigned spins = 0;; ++spins) {
    g = __hip_atomic_load(gran + lane, RLX_AGENT);
    if (__all((unsigned)(g >> 32) == ep)) break;
    if (spins > SPIN_CAP) break;
    __builtin_amdgcn_s_sleep(1);
  }
  const float val = __uint_as_float((unsigned)g);
  const float qk = dpp0<0x101>(val);
  float mean, rstd;
  gn_combine(val, qk, !(lane & 1), 16384.f, mean, rstd);
  if (lane == 0) { red[16] = mean; red[17] = rstd; }
}

DI void put_row(char* lds, __amdgpu_buffer_rsrc_t rv, const Tile& T, int ho, int rho, bool publish, int i16, int kg, const u32x4 (&o)[4]) {
  const int pos = i16 * 64 + vpos(kg, i16) * 16;
  char* d = lds + 2 * ho * VSLOT + rho * 1024 + pos;
#pragma unroll
  for (int xi = 0; xi < 4; ++xi) *(u32x4*)(d + (xi & 1) * VSLOT + (xi >> 1) * 10240) = o[xi];
  if (publish) {
    const int so = (T.b * 16 + 2 * T.nt + ho) * 524288 + (T.y0 - 1 + rho) * 2048 + T.tx * 1024;
#pragma unroll
    for (int xi = 0; xi < 4; ++xi)
      __builtin_amdgcn_raw_buffer_store_b128(o[xi], rv, pos + so + (xi & 1) * 262144 + (xi >> 1) * 131072, 0, 16);
  }
}

template <int NR> struct XRows { unsigned v[NR][8]; unsigned short col[2]; u32x4 gv, bv; unsigned short gc, bc; };
DI int clamp_y(int y) { return min(max(y, 0), 63); }
template <int NR> DI void rows_load(XRows<NR>& X, const int (&rho)[NR], int rc0, int rc1, const u16* __restrict__ x, const u16* __restrict__ gam,
                                    const u16* __restrict__ bet, const Tile& T, int ho, int lane) {
  const int i16 = lane & 15, kg = lane >> 4, chl = lane & 31;
  const u16* xb = x + (size_t)(T.b * NCH + T.nt * 64 + 32 * ho) * HW;
#pragma unroll
  for (int k = 0; k < NR; ++k)
#pragma unroll
    for (int j = 0; j < 8; ++j) X.v[k][j] = *(const unsigned*)(xb + (size_t)(8 * kg + j) * HW + clamp_y(T.y0 - 1 + rho[k]) * 64 + 32 * T.tx + 2 * i16);
  const int xc = T.tx == 0 ? 32 : 31;
  X.col[0] = xb[(size_t)chl * HW + clamp_y(T.y0 - 1 + rc0) * 64 + xc];
  X.col[1] = xb[(size_t)chl * HW + clamp_y(T.y0 - 1 + rc1) * 64 + xc];
  const int ch = T.nt * 64 + 32 * ho;
  X.gv = *(const u32x4*)(gam + ch + 8 * kg); X.bv = *(const u32x4*)(bet + ch + 8 * kg);
  X.gc = gam[ch + chl]; X.bc = bet[ch + chl];
}
template <int NR, int NI> DI void rows_stats(const XRows<NR>& X, float* red, int wid, int lane) {
  const float piv = __builtin_bit_cast(float, __builtin_amdgcn_readfirstlane(X.v[0][0] << 16));
  f32x2 sv = {0.f, 0.f}, qv = {0.f, 0.f};
#pragma unroll
  for (int k = 0; k < NI; ++k)
#pragma unroll
    for (int j = 0; j < 8; ++j) {
      const f32x2 d = f32x2{bf_lo(X.v[k][j]), bf_hi(X.v[k][j])} - piv;
      sv += d; qv = __builtin_elementwise_fma(d, d, qv);
    }
  const float s = wave_sum(sv[0] + sv[1]), q = wave_sum(qv[0] + qv[1]);
  if (lane == 0) { red[wid] = s + piv * (float)(NI * 1024); red[8 + wid] = q - s * s * (1.f / (float)(NI * 1024)); }
}
template <int NR> DI void rows_emit(const XRows<NR>& X, const int (&rho)[NR], int k0, int k1, char* lds, char* ws, const Tile& T, int wid, int ho, int lane) {
  const int i16 = lane & 15, kg = lane >> 4;
  const float* red = (const float*)(lds + M_RED);
  const float mean = red[16], rstd = red[17];
  float sa[8], sb[8];
#pragma unroll
  for (int j = 0; j < 8; ++j) {
    sa[j] = rstd * ((j & 1) ? bf_hi(X.gv[j >> 1]) : bf_lo(X.gv[j >> 1]));
    sb[j] = ((j & 1) ? bf_hi(X.bv[j >> 1]) : bf_lo(X.bv[j >> 1])) - mean * sa[j];
  }
  const float* col = (const float*)(lds + M_COL + wid * 512);
  const __amdgpu_buffer_rsrc_t rv = __builtin_amdgcn_make_buffer_rsrc((void*)(ws + OFF_V1), (short)0, (int)V_BYTES, 0x00020000);
#pragma unroll
  for (int k = 0; k < NR; ++k) {
    if (k < k0 || k >= k1) continue;
    f32x4 hl[2], hr[2];
    halo_ops(lds, col + k * 32, T.tx, i16, kg, hl, hr);
    float t[4][8];
#pragma unroll
    for (int j = 0; j < 8; ++j) {
      const f32x2 a2 = {sa[j], sa[j]}, b2 = {sb[j], sb[j]};
      const f32x2 d = silu_aff2(f32x2{bf_lo(X.v[k][j]), bf_hi(X.v[k][j])}, a2, b2, a2 * -1.44269504088896341f, b2 * -1.44269504088896341f);
      t[0][j] = dpp_prev(d[1]) - (d[1] - hl[j >> 2][j & 3]);
      t[1][j] = d[0] + d[1];
      t[2][j] = d[1] - d[0];
      t[3][j] = (d[0] - hr[j >> 2][j & 3]) - dpp_next(d[0]);
    }
    u32x4 o[4];
#pragma unroll
    for (int xi = 0; xi < 4; ++xi)
      o[xi] = u32x4{pack_bf16(t[xi][0], t[xi][1]), pack_bf16(t[xi][2], t[xi][3]), pack_bf16(t[xi][4], t[xi][5]), pack_bf16(t[xi][6], t[xi][7])};
    const int y = T.y0 - 1 + rho[k];
    const bool halo_row = NR == 2 && k == 1;
    if (halo_row && (y < 0 || y > 63)) { o[0] = o[1] = o[2] = o[3] = u32x4{0u, 0u, 0u, 0u}; }
    put_row(lds, rv, T, ho, rho[k], !halo_row, i16, kg, o);
  }
}
template <int NR> DI void rows_column(const XRows<NR>& X, char* lds, int wid, int lane) {
  const int chl = lane & 31, half = lane >> 5;
  const float* red = (const float*)(lds + M_RED);
  const float mean = red[16], rstd = red[17];
  float* col = (float*)(lds + M_COL + wid * 512);
  const float sa = rstd * __uint_as_float((unsigned)X.gc << 16), sb = __uint_as_float((unsigned)X.bc << 16) - mean * sa;
  col[lane] = silu_f(__uint_as_float((unsigned)X.col[0] << 16) * sa + sb);
  if constexpr (NR == 3) {
    const float v2 = silu_f(__uint_as_float((unsigned)X.col[1] << 16) * sa + sb);
    if (half == 0) col[64 + chl] = v2;
  }
}

DI int u_row(int conv, int of, int i) { return conv == 0 ? (32 * (of >> 1) + 8 * (i >> 2) + 4 * (of & 1) + (i & 3)) : (16 * of + i); }
DI void u_load(u32x4 (&d)[9], const u16* __restrict__ w, int conv, const Tile& T, int uidx, int of, int lane) {
  const int i = lane & 15, kg = lane >> 4;
  const u32x4* src = (const u32x4*)(w + ((size_t)(T.nt * 64 + u_row(conv, of, i)) * 512 + 32 * (uidx >> 1) + 8 * kg) * 9);
#pragma unroll
  for (int k = 0; k < 9; ++k) d[k] = src[k];
}
DI void u_stage(unsigned ldsdst, const u16* __restrict__ w, int conv, const Tile& T, int uidx, int of, int lane) {
  const __amdgpu_buffer_rsrc_t rw = __builtin_amdgcn_make_buffer_rsrc((void*)w, (short)0, 512 * 512 * 9 * 2, 0x00020000);
#pragma unroll
  for (int k = 0; k < 9; ++k) {
    const int t = k * 64 + lane, row = t / 36, c16 = t - row * 36;
    dma1(ldsdst + k * 1024, (unsigned)(((T.nt * 64 + u_row(conv, of, row)) * 512 + 32 * (uidx >> 1)) * 18 + c16 * 16), rw, 0u);
  }
}
DI void u_fetch(u32x4 (&d)[9], const char* stage, int lane) {
#pragma unroll
  for (int k = 0; k < 9; ++k) d[k] = *(const u32x4*)(stage + (lane & 15) * 576 + (lane >> 4) * 144 + k * 16);
}
DI void u_emit(const u32x4 (&d)[9], char* ws, int conv, const Tile& T, int uidx, int of, int lane) {
  const __amdgpu_buffer_rsrc_t ru = __builtin_amdgcn_make_buffer_rsrc((void*)(ws + (conv == 0 ? OFF_U1 : OFF_U2)), (short)0, (int)U_BYTES, 0x00020000);
  const int dst = T.nt * 786432 + uidx * USLOT + of * 1024 + lane * 16;
  const float sg = (uidx & 1) ? 1.f : -1.f;
#pragma unroll
  for (int dy = 0; dy < 3; ++dy) {
    float ua[8], ub[8];
#pragma unroll
    for (int j = 0; j < 8; ++j) {
      float g[3];
#pragma unroll
      for (int dx = 0; dx < 3; ++dx) {
        const int e = 9 * j + 3 * dy + dx;
        const unsigned dw = d[e >> 3][(e >> 1) & 3];
        g[dx] = (e & 1) ? bf_hi(dw) : bf_lo(dw);
      }
      const float mid = 0.5f * (g[0] + sg * g[1] + g[2]);
      ua[j] = (uidx & 1) ? mid : g[0];
      ub[j] = (uidx & 1) ? g[2] : mid;
    }
    const u32x4 oa = {pack_bf16(ua[0], ua[1]), pack_bf16(ua[2], ua[3]), pack_bf16(ua[4], ua[5]), pack_bf16(ua[6], ua[7])};
    const u32x4 ob = {pack_bf16(ub[0], ub[1]), pack_bf16(ub[2], ub[3]), pack_bf16(ub[4], ub[5]), pack_bf16(ub[6], ub[7])};
    __builtin_amdgcn_raw_buffer_store_b128(oa, ru, dst + dy * 4096, 0, 16);
    __builtin_amdgcn_raw_buffer_store_b128(ob, ru, dst + 12288 + dy * 4096, 0, 16);
  }
}

template <int P> DI void epi1_mfma(const Comp& s, char* lds, char* ws, const Tile& T, unsigned ep, int wid, int lane) {
  constexpr int MODE = 1; (void)MODE;
  const int i16 = lane & 15, kg = lane >> 4, ho = wid >> 1;
  f32x4 ye[4][2], yo[4][2];
  exchange<P>(s, lds, wid, lane, ye, yo);
  if (wid == 0) { STAMP(12); }
  {
    const f32x4 e0 = *(const f32x4*)(lds + M_EMB + (32 * ho + 8 * kg) * 4), e1 = *(const f32x4*)(lds + M_EMB + (32 * ho + 8 * kg) * 4 + 16);
#pragma unroll
    for (int r = 0; r < 4; ++r) { ye[r][0] += e0; ye[r][1] += e1; yo[r][0] += e0; yo[r][1] += e1; }
  }
  const __amdgpu_buffer_rsrc_t rh = __builtin_amdgcn_make_buffer_rsrc((void*)(ws + OFF_HALO), (short)0, 256 * HALO_STRIDE, 0x00020000);
  float* red = (float*)(lds + M_RED);
  const int hb = T.bid * HALO_STRIDE;
  constexpr int RE = P == 0 ? 0 : 3;
#pragma unroll
  for (int f = 0; f < 2; ++f) {
    __builtin_amdgcn_raw_buffer_store_b128(__builtin_bit_cast(u32x4, ye[RE][f]), rh, hb + P * 8192 + ((ho * 2 + f) * 2) * 1024 + lane * 16, 0, 16);
    __builtin_amdgcn_raw_buffer_store_b128(__builtin_bit_cast(u32x4, yo[RE][f]), rh, hb + P * 8192 + ((ho * 2 + f) * 2 + 1) * 1024 + lane * 16, 0, 16);
  }
  if (i16 == (T.tx == 0 ? 15 : 0)) {
#pragma unroll
    for (int r = 0; r < 4; ++r)
#pragma unroll
      for (int f = 0; f < 2; ++f)
        __builtin_amdgcn_raw_buffer_store_b128(__builtin_bit_cast(u32x4, T.tx == 0 ? yo[r][f] : ye[r][f]), rh,
                                               hb + 16384 + (4 * P + r) * 256 + (32 * ho + 8 * kg + 4 * f) * 4, 0, 16);
  }
  {
    const float piv = __builtin_bit_cast(float, __builtin_amdgcn_readfirstlane(__builtin_bit_cast(int, ye[0][0][0])));
    f32x4 sv = {0.f, 0.f, 0.f, 0.f}, qv = {0.f, 0.f, 0.f, 0.f};
#pragma unroll
    for (int r = 0; r < 4; ++r)
#pragma unroll
      for (int f = 0; f < 2; ++f) {
        const f32x4 d0 = ye[r][f] - piv, d1 = yo[r][f] - piv;
        sv += d0 + d1;
        qv = __builtin_elementwise_fma(d0, d0, qv);
        qv = __builtin_elementwise_fma(d1, d1, qv);
      }
    const float sum = wave_sum((sv[0] + sv[1]) + (sv[2] + sv[3])), q = wave_sum((qv[0] + qv[1]) + (qv[2] + qv[3]));
    if (lane == 0) { red[wid] = sum + piv * 4096.f; red[8 + wid] = q - sum * sum * (1.f / 4096.f); }
  }
  constexpr int RH = P == 0 ? 3 : 0;
  char* hd = lds + X_HAND + wid * 4096 + lane * 16;
#pragma unroll
  for (int f = 0; f < 2; ++f) { *(f32x4*)(hd + (f * 2) * 1024) = ye[RH][f]; *(f32x4*)(hd + (f * 2 + 1) * 1024) = yo[RH][f]; }
  lds_bar();
  if (wid == 0) { STAMP(13); }
  wait_vm<0>();
  set_tag(ws, T.bid, W_HALO + wid, ep, lane);
  const int chl = lane & 31, half = lane >> 5;
  const float gam_c = ((const float*)(lds + M_GAM))[32 * ho + chl], bet_c = ((const float*)(lds + M_BET))[32 * ho + chl];
  lds_bar();
  if (wid == 0) { STAMP(11); }
  Affine af;
  load_affine(af, lds, ho, kg);
  float* col = (float*)(lds + M_COL + wid * 512);
  {
    const float* raw = (const float*)(lds + M_RAW);
    const float sa = red[17] * gam_c, sb = bet_c - red[16] * sa;
    col[lane] = silu_f(raw[(5 * P + 1 + half) * 64 + 32 * ho + chl] * sa + sb);
    const float v2 = silu_f(raw[(5 * P + 3) * 64 + 32 * ho + chl] * sa + sb);
    if (half == 0) col[64 + chl] = v2;
  }
  if (wid == 0) { STAMP(18); }
  const __amdgpu_buffer_rsrc_t rv = __builtin_amdgcn_make_buffer_rsrc((void*)(ws + OFF_V2), (short)0, (int)V_BYTES, 0x00020000);
#pragma unroll
  for (int k = 0; k < 3; ++k) {
    constexpr int R0 = P == 0 ? 0 : 1;
    f32x4 hl[2], hr[2];
    halo_ops(lds, col + k * 32, T.tx, i16, kg, hl, hr);
    u32x4 o[4];
    row_transform(ye[R0 + k], yo[R0 + k], hl, hr, af, o);
    put_row(lds, rv, T, ho, 4 * P + R0 + k + 1, true, i16, kg, o);
  }
  if (wid == 0) { STAMP(19); }
}

DI void epi1_loader(const Loader& L, int uprod, char* lds, char* ws, const Tile& T, unsigned ep, int wid, int lane) {
  constexpr int MODE = 1; (void)MODE;
  const int i16 = lane & 15, kg = lane >> 4, p = wid & 1, ho = (wid >> 1) & 1;
  const __amdgpu_buffer_rsrc_t rh = __builtin_amdgcn_make_buffer_rsrc((void*)(ws + OFF_HALO), (short)0, 256 * HALO_STRIDE, 0x00020000);
  float* red = (float*)(lds + M_RED);
  lds_bar();
  bar();
  wait_tags<1>(ws, uprod, W_U2, lane < 32, ep);
  ld_u(L, 0); ld_u(L, 1); ld_u(L, 2);
  bar();
  if (wid == 4) {
    const float s0 = red[0], s1 = red[1], s2 = red[2], s3 = red[3];
    const float tot = (s0 + s1) + (s2 + s3), bm = tot * (1.f / 16384.f);
    const float d0 = s0 * (1.f / 4096.f) - bm, d1 = s1 * (1.f / 4096.f) - bm, d2 = s2 * (1.f / 4096.f) - bm, d3 = s3 * (1.f / 4096.f) - bm;
    const float m2 = (red[8] + red[9]) + (red[10] + red[11]) + 4096.f * ((d0 * d0 + d1 * d1) + (d2 * d2 + d3 * d3));
    group_stats(ws, 1, T, tot, m2, ep, red, lane);
    STAMP(14); STAMPR(16);
    group_stats_wait(ws, 1, T, ep, red, lane);
    STAMP(15); STAMPR(17);
  }
  const int tyn = p == 0 ? T.ty - 1 : T.ty + 1;
  const bool have_v = tyn >= 0 && tyn < 8;
  const int bid_v = tile_bid(T.b, have_v ? tyn : T.ty, T.tx, T.nt);
  f32x4 ve[2], vo[2];
  const int hbv = bid_v * HALO_STRIDE + (p == 0 ? 8192 : 0) + ho * 4096 + lane * 16;
  if (wid != 4) {
    wait_tags<1>(ws, bid_v, W_HALO, true, ep);
#pragma unroll
    for (int f = 0; f < 2; ++f) {
      ve[f] = __builtin_bit_cast(f32x4, __builtin_amdgcn_raw_buffer_load_b128(rh, hbv + f * 2048, 0, 16));
      vo[f] = __builtin_bit_cast(f32x4, __builtin_amdgcn_raw_buffer_load_b128(rh, hbv + f * 2048 + 1024, 0, 16));
    }
  }
  if (wid == 5) {
    const int nb = T.bid ^ 32;
    wait_tags<1>(ws, nb, W_HALO, true, ep);
    const u32x4 c0 = __builtin_amdgcn_raw_buffer_load_b128(rh, nb * HALO_STRIDE + 16384 + lane * 16, 0, 16);
    const u32x4 c1 = __builtin_amdgcn_raw_buffer_load_b128(rh, nb * HALO_STRIDE + 16384 + 1024 + lane * 16, 0, 16);
    *(u32x4*)(lds + M_RAW + 256 + lane * 16) = c0;
    *(u32x4*)(lds + M_RAW + 256 + 1024 + lane * 16) = c1;
  } else if (wid >= 6) {
    const int tyd = wid == 6 ? T.ty - 1 : T.ty + 1;
    float cv = 0.f;
    if (tyd >= 0 && tyd < 8) {
      const int nb = tile_bid(T.b, tyd, T.tx ^ 1, T.nt);
      wait_tags<1>(ws, nb, W_HALO, true, ep);
      cv = __uint_as_float(__builtin_amdgcn_raw_buffer_load_b32(rh, nb * HALO_STRIDE + 16384 + (wid == 6 ? 7 : 0) * 256 + lane * 4, 0, 16));
    }
    ((float*)(lds + M_RAW))[(wid == 6 ? 0 : 9) * 64 + lane] = cv;
  }
  const int chl = lane & 31, half = lane >> 5;
  const float gam_c = ((const float*)(lds + M_GAM))[32 * ho + chl], bet_c = ((const float*)(lds + M_BET))[32 * ho + chl];
  f32x4 he[2], hodd[2];
  const char* hd = lds + X_HAND + (wid - 4) * 4096 + lane * 16;
#pragma unroll
  for (int f = 0; f < 2; ++f) { he[f] = *(const f32x4*)(hd + (f * 2) * 1024); hodd[f] = *(const f32x4*)(hd + (f * 2 + 1) * 1024); }
  lds_bar();
  wait_vm<0>();
  if (wid == 4) {
    wait_tags<1>(ws, bid_v, W_HALO, true, ep);
#pragma unroll
    for (int f = 0; f < 2; ++f) {
      ve[f] = __builtin_bit_cast(f32x4, __builtin_amdgcn_raw_buffer_load_b128(rh, hbv + f * 2048, 0, 16));
      vo[f] = __builtin_bit_cast(f32x4, __builtin_amdgcn_raw_buffer_load_b128(rh, hbv + f * 2048 + 1024, 0, 16));
    }
  }
  Affine af;
  load_affine(af, lds, ho, kg);
  float* col = (float*)(lds + M_COL + wid * 512);
  {
    const float* raw = (const float*)(lds + M_RAW);
    const float sa = red[17] * gam_c, sb = bet_c - red[16] * sa;
    col[lane] = silu_f(raw[(half == 0 ? 4 + p : 9 * p) * 64 + 32 * ho + chl] * sa + sb);
  }
  const __amdgpu_buffer_rsrc_t rv = __builtin_amdgcn_make_buffer_rsrc((void*)(ws + OFF_V2), (short)0, (int)V_BYTES, 0x00020000);
  f32x4 hl[2], hr[2];
  u32x4 o[4];
  halo_ops(lds, col, T.tx, i16, kg, hl, hr);
  row_transform(he, hodd, hl, hr, af, o);
  put_row(lds, rv, T, ho, 4 + p, true, i16, kg, o);
  halo_ops(lds, col + 32, T.tx, i16, kg, hl, hr);
  row_transform(ve, vo, hl, hr, af, o);
  if (!have_v) { o[0] = o[1] = o[2] = o[3] = u32x4{0u, 0u, 0u, 0u}; }
  put_row(lds, rv, T, ho, 9 * p, false, i16, kg, o);
}

constexpr int xpiece(int k) { return k < 24 ? LDS_U + 2 * USLOT + k * 1024 : LDS_U + (k - 24) * 1024; }

template <int P> DI void epi2_mfma(const Comp& s, char* lds, char* ws, const Tile& T, u16* __restrict__ out, int wid, int lane) {
  constexpr int MODE = 2; (void)MODE; (void)ws;
  const int i16 = lane & 15, kg = lane >> 4, ho = wid >> 1;
  bar();
  f32x4 ye[4][2], yo[4][2];
  exchange<P>(s, lds, wid, lane, ye, yo);
  if (wid == 0) { STAMP(12); }
  const size_t off0 = ((size_t)(T.b * NCH + T.nt * 64 + 32 * ho + i16)) * HW + (T.y0 + 4 * P) * 64 + 32 * T.tx + 8 * kg;
#pragma unroll
  for (int f = 0; f < 2; ++f)
#pragma unroll
    for (int r = 0; r < 4; ++r) {
      const int k = (ho * 2 + f) * 8 + 4 * P + r;
      const u32x4 xv = *(const u32x4*)(lds + (k < 24 ? LDS_U + 2 * USLOT + k * 1024 : LDS_U + (k - 24) * 1024) + lane * 16);
      u32x4 o;
#pragma unroll
      for (int j = 0; j < 4; ++j) o[j] = pack_bf16(ye[r][f][j] + bf_lo(xv[j]), yo[r][f][j] + bf_hi(xv[j]));
      *(u32x4*)(out + off0 + (size_t)f * 16 * HW + r * 64) = o;
    }
}

template <int MODE> DI void loader_loop(const Loader& L, const u32x4 (&dw2)[9], int uidx, char* ws, const Tile& T, unsigned ep, int w, int lane) {
  lds_bar();
  if (w == 0) { STAMPR(20); }
  bar();
  ld_u(L, 3);
  wait_vm<6>();
  set_tag(ws, T.bid, (MODE == 1 ? W_V1 : W_V2) + 4 + w, ep, lane);
  bar();
  ld_u(L, 4);
  wait_vm<6>();
  bar();
  if constexpr (MODE == 1) u_emit(dw2, ws, 1, T, uidx, w, lane);
  {
    if (w == 0) { STAMPR(22); }
    const int ty = T.ty - 1 + (lane >> 3);
    const bool need = lane < 24 && ty >= 0 && ty < 8;
    wait_tags<2>(ws, need ? tile_bid(T.b, ty, T.tx, lane & 7) : T.bid, MODE == 1 ? W_V1 : W_V2, need, ep);
    if (w == 0) { STAMPR(23); }
  }
  ld_v(L, 4); ld_v(L, 5); ld_u(L, 5);
  wait_vm<11>();
  if constexpr (MODE == 1) set_tag(ws, T.bid, W_U2 + w, ep, lane);
  bar();
#pragma unroll 1
  for (int t = 4; t < NUNIT - 2; ++t) {
    ld_v(L, t + 2); ld_u(L, t + 2);
    wait_vm<11>();
    bar();
  }
}

template <int MODE> DI void mfma_loop(Comp& s, char* ws, int bid, unsigned ep, int wid, int lane) {
  lds_bar();
  lds_rd<0>(s.B[0][0], s.bbase); lds_rd<1024>(s.B[0][1], s.bbase); lds_rd<0>(s.A[0], s.abase);
  lds_rd<4096>(s.B[0][2], s.bbase); lds_rd<5120>(s.B[0][3], s.bbase); lds_rd<8192>(s.B[0][4], s.bbase); lds_rd<9216>(s.B[0][5], s.bbase);
  lds_rd<1024>(s.A[1], s.abase); lds_rd<2048>(s.A[2], s.abase); lds_rd<3072>(s.A[3], s.abase);
  wait_lgkm<3>();
  s.anxt = s.abase;
  unsigned voff = 0, uoff = 0;
#pragma unroll 1
  for (int c = 0; c < 16; ++c) {
    unit<MODE, 0>(s, voff, uoff);
    if (c == 0) {
      wait_vm<0>();
      set_tag(ws, bid, (MODE == 1 ? W_V1 : W_V2) + wid, ep, lane);
    }
    unit<MODE, 1>(s, voff, uoff);
  }
  asm volatile("s_waitcnt lgkmcnt(0)\n\ts_nop 15" ::: "memory");
}
DI void clear_acc(Comp& s) {
#pragma unroll
  for (int e = 0; e < 2; ++e)
#pragma unroll
    for (int r = 0; r < 8; ++r)
#pragma unroll
      for (int o = 0; o < 2; ++o) { s.acc[e][r][o] = f32x4{0.f, 0.f, 0.f, 0.f}; asm volatile("" : "+v"(s.acc[e][r][o])); }
}

__global__ __launch_bounds__(512, 2) void k_main(const u16* __restrict__ x, const u16* __restrict__ temb, const u16* __restrict__ g1, const u16* __restrict__ be1,
                                                 const u16* __restrict__ w1, const u16* __restrict__ wemb, const u16* __restrict__ g2,
                                                 const u16* __restrict__ be2, const u16* __restrict__ w2, u16* __restrict__ out, char* __restrict__ ws) {
  __shared__ __attribute__((aligned(16))) char lds[LDS_TOTAL];
  const int tid = threadIdx.x, lane = tid & 63;
  const int wid = __builtin_amdgcn_readfirstlane(tid >> 6);
  const int bid = blockIdx.x, xcd = bid & 7, slot = bid >> 3;
  Tile T;
  T.bid = bid; T.b = xcd >> 2; T.nt = (xcd & 1) * 4 + (slot & 3); T.ty = ((xcd >> 1) & 1) * 4 + (slot >> 3); T.tx = (slot >> 2) & 1; T.y0 = T.ty * 8;
  const unsigned ldsb = (unsigned)(size_t)lds;
  const int p = wid & 1, ho = (wid >> 1) & 1;
  const unsigned ep = __builtin_amdgcn_readfirstlane(__hip_atomic_load((gu32*)(ws + OFF_EPOCH), RLX_AGENT)) + 1u;
  float* red = (float*)(lds + M_RED);

  if (wid >= 4) {
    const int w = wid - 4;
    const int uidx = (T.b * 8 + T.ty) * 2 + T.tx;
    const int rho[2] = {4 + p, 9 * p};
    const char* stage = lds + LDS_U + w * 9216;
    u_stage(ldsb + LDS_U + w * 9216, w1, 0, T, uidx, w, lane);
    XRows<2> X;
    rows_load<2>(X, rho, (lane >> 5) ? 9 * p : 4 + p, 0, x, g1, be1, T, ho, lane);
    u32x4 we[3];
    unsigned short te[2][3];
    if (w == 3) {
#pragma unroll
      for (int i = 0; i < 3; ++i) {
        const int k = 24 * uidx + 3 * (lane >> 3) + i;
        we[i] = *(const u32x4*)(wemb + (size_t)k * 512 + T.nt * 64 + 8 * (lane & 7));
        te[0][i] = temb[k]; te[1][i] = temb[NEMB + k];
      }
    }
    rows_stats<2, 1>(X, red, wid, lane);
    { constexpr int MODE = 1; (void)MODE; if (w == 1) { STAMPR(48); } }
    lds_bar();
    { constexpr int MODE = 1; (void)MODE; if (w == 1) { STAMPR(49); } }
    {
      u32x4 dw1[9];
      u_fetch(dw1, stage, lane);
      u_emit(dw1, ws, 0, T, uidx, w, lane);
    }
    { constexpr int MODE = 1; (void)MODE; if (w == 1) { STAMPR(50); } }
    if (w == 3) {
      float a[2][8];
#pragma unroll
      for (int j = 0; j < 8; ++j) a[0][j] = a[1][j] = 0.f;
#pragma unroll
      for (int i = 0; i < 3; ++i) {
        const float t0 = silu_f(__uint_as_float((unsigned)te[0][i] << 16)), t1 = silu_f(__uint_as_float((unsigned)te[1][i] << 16));
#pragma unroll
        for (int j = 0; j < 8; ++j) {
          const float wv = (j & 1) ? bf_hi(we[i][j >> 1]) : bf_lo(we[i][j >> 1]);
          a[0][j] += t0 * wv; a[1][j] += t1 * wv;
        }
      }
      float* sc = (float*)(lds + M_COL);
#pragma unroll
      for (int j = 0; j < 8; ++j) { sc[(lane >> 3) * 128 + (lane & 7) * 8 + j] = a[0][j]; sc[(lane >> 3) * 128 + 64 + (lane & 7) * 8 + j] = a[1][j]; }
      float e0 = 0.f, e1 = 0.f;
#pragma unroll
      for (int g = 0; g < 8; ++g) { e0 += sc[g * 128 + lane]; e1 += sc[g * 128 + 64 + lane]; }
      const __amdgpu_buffer_rsrc_t re = __builtin_amdgcn_make_buffer_rsrc((void*)(ws + OFF_EMBP), (short)0, 8 * 32 * 512, 0x00020000);
      __builtin_amdgcn_raw_buffer_store_b32(__float_as_uint(e0), re, (T.nt * 32 + uidx) * 512 + lane * 4, 0, 16);
      __builtin_amdgcn_raw_buffer_store_b32(__float_as_uint(e1), re, (T.nt * 32 + uidx) * 512 + 256 + lane * 4, 0, 16);
    }
    wait_vm<0>();
    set_tag(ws, T.bid, W_U1 + w, ep, lane);
    { constexpr int MODE = 1; (void)MODE; if (w == 1) { STAMPR(52); } }
    lds_bar();
    Loader L;
    L.nt = T.nt;
    L.rv = __builtin_amdgcn_make_buffer_rsrc((void*)(ws + OFF_V1), (short)0, (int)V_BYTES, 0x00020000);
    L.ru = __builtin_amdgcn_make_buffer_rsrc((void*)(ws + OFF_U1), (short)0, (int)U_BYTES, 0x00020000);
    L.vmid = lane * 16;
    L.vfirst = (T.y0 == 0 && !(w & 1)) ? 0x80000000u : L.vmid;
    L.vlast = (T.y0 == 56 && (w & 1)) ? 0x80000000u : L.vmid;
    L.sv = (unsigned)T.b * 16u * 524288u + (unsigned)(w >> 1) * 131072u + (unsigned)((T.y0 + 5 * (w & 1)) * 2048 + T.tx * 1024) - 2048u;
    L.su = (unsigned)T.nt * 786432u + (unsigned)w * 6144u;
    L.mv = ldsb + (w >> 1) * 10240 + (w & 1) * 5120;
    L.mu = ldsb + LDS_U + w * 6144;
    const int uprod = tile_bid((lane >> 4) & 1, (lane >> 1) & 7, lane & 1, T.nt);
    rows_column<2>(X, lds, wid, lane);
    wait_tags<1>(ws, uprod, W_U1, lane < 32, ep);
    { constexpr int MODE = 1; (void)MODE; if (w == 1) { STAMPR(53); } }
    u32x4 dw2[9];
    u_load(dw2, w2, 1, T, uidx, w, lane);
    u32x4 ep8[8];
    if (w == 3) {
      const __amdgpu_buffer_rsrc_t re = __builtin_amdgcn_make_buffer_rsrc((void*)(ws + OFF_EMBP), (short)0, 8 * 32 * 512, 0x00020000);
#pragma unroll
      for (int i = 0; i < 8; ++i) ep8[i] = __builtin_amdgcn_raw_buffer_load_b128(re, (T.nt * 32 + (lane >> 4) * 8 + i) * 512 + T.b * 256 + (lane & 15) * 16, 0, 16);
    }
    ld_u(L, 0); ld_u(L, 1); ld_u(L, 2);
    rows_emit<2>(X, rho, 0, 2, lds, ws, T, wid, ho, lane);
    wait_vm<4>();
    loader_loop<1>(L, dw2, uidx, ws, T, ep, w, lane);
    if (w == 3) {
      f32x4 e = {0.f, 0.f, 0.f, 0.f};
#pragma unroll
      for (int i = 0; i < 8; ++i) e += __builtin_bit_cast(f32x4, ep8[i]);
#pragma unroll
      for (int j = 0; j < 4; ++j) { e[j] += __shfl_xor(e[j], 16); e[j] += __shfl_xor(e[j], 32); }
      if (lane < 16) *(f32x4*)(lds + M_EMB + lane * 16) = e;
    }
    wait_vm<0>();
    bar();
    bar();
    L.rv = __builtin_amdgcn_make_buffer_rsrc((void*)(ws + OFF_V2), (short)0, (int)V_BYTES, 0x00020000);
    L.ru = __builtin_amdgcn_make_buffer_rsrc((void*)(ws + OFF_U2), (short)0, (int)U_BYTES, 0x00020000);
    epi1_loader(L, uprod, lds, ws, T, ep, wid, lane);
    loader_loop<2>(L, dw2, uidx, ws, T, ep, w, lane);
    {
      const __amdgpu_buffer_rsrc_t rx = __builtin_amdgcn_make_buffer_rsrc((void*)x, (short)0, 2 * NCH * HW * 2, 0x00020000);
      const unsigned vx = (lane & 15) * (HW * 2) + (lane >> 4) * 16;
      const unsigned sx = (unsigned)((T.b * NCH + T.nt * 64) * HW + T.y0 * 64 + 32 * T.tx) * 2u;
#pragma unroll
      for (int q = 0; q < 6; ++q) { const int k = w * 6 + q; dma1(ldsb + xpiece(0) + k * 1024, vx, rx, sx + (unsigned)(k >> 3) * 16u * HW * 2u + (unsigned)(k & 7) * 128u); }
      wait_vm<6>();
      bar();
#pragma unroll
      for (int q = 0; q < 2; ++q) { const int k = 24 + w * 2 + q; dma1(ldsb + xpiece(24) + (k - 24) * 1024, vx, rx, sx + (unsigned)(k >> 3) * 16u * HW * 2u + (unsigned)(k & 7) * 128u); }
      bar();
      wait_vm<0>();
      bar();
    }
    return;
  }

  const int i16 = lane & 15, kg = lane >> 4;
  {
    constexpr int MODE = 1; (void)MODE;
    if (wid == 0) { STAMP(0); STAMPR(4); }
    const int rho[3] = {5 * p + 1, 5 * p + 2, 5 * p + 3};
    XRows<3> X;
    rows_load<3>(X, rho, 5 * p + 1 + (lane >> 5), 5 * p + 3, x, g1, be1, T, ho, lane);
    if (wid == 0) {
      const int ch = T.nt * 64 + lane;
      ((float*)(lds + M_GAM))[lane] = __uint_as_float((unsigned)g2[ch] << 16);
      ((float*)(lds + M_BET))[lane] = __uint_as_float((unsigned)be2[ch] << 16);
      if (lane < 8) ((float*)(lds + M_ZERO))[lane] = 0.f;
    }
    rows_stats<3, 3>(X, red, wid, lane);
    if (wid == 0) { STAMP(24); }
    lds_bar();
    if (wid == 0) {
      float tot = 0.f;
#pragma unroll
      for (int k = 0; k < 8; ++k) tot += red[k];
      const float bm = tot * (1.f / 16384.f);
      float m2 = 0.f;
#pragma unroll
      for (int k = 0; k < 8; ++k) {
        const float n = k < 4 ? 3072.f : 1024.f, d = red[k] * (k < 4 ? 1.f / 3072.f : 1.f / 1024.f) - bm;
        m2 += red[8 + k] + n * d * d;
      }
      group_stats(ws, 0, T, tot, m2, ep, red, lane);
      group_stats_wait(ws, 0, T, ep, red, lane);
      STAMPR(54);
    }
    lds_bar();
    if (wid == 0) { STAMP(25); }
    rows_column<3>(X, lds, wid, lane);
    rows_emit<3>(X, rho, 0, 3, lds, ws, T, wid, ho, lane);
  }
  Comp s;
  s.abase = ldsb + p * 10240 + i16 * 64 + vpos(kg, i16) * 16;
  s.bbase = ldsb + LDS_U + p * 12288 + ho * 2048 + lane * 16;
  clear_acc(s);
  {
    constexpr int MODE = 1; (void)MODE;
    if (wid == 0) { STAMP(1); }
    mfma_loop<1>(s, ws, bid, ep, wid, lane);
    if (wid == 0) { STAMP(2); STAMPR(5); }
    bar();
    if (p == 0) epi1_mfma<0>(s, lds, ws, T, ep, wid, lane); else epi1_mfma<1>(s, lds, ws, T, ep, wid, lane);
  }
  {
    constexpr int MODE = 2; (void)MODE;
    if (wid == 0) { STAMP(0); STAMPR(4); }
    clear_acc(s);
    if (wid == 0) { STAMP(1); }
    mfma_loop<2>(s, ws, bid, ep, wid, lane);
    if (wid == 0) { STAMP(2); STAMPR(5); }
    if (p == 0) epi2_mfma<0>(s, lds, ws, T, out, wid, lane); else epi2_mfma<1>(s, lds, ws, T, out, wid, lane);
  }
  if (bid == 0 && tid == 0) __hip_atomic_store((gu32*)(ws + OFF_EPOCH), ep, RLX_AGENT);
}

extern "C" void kernel_launch(void* const* d_in, const int* in_sizes, int n_in, void* d_out, int out_size, void* d_ws, size_t ws_size,
                              hipStream_t stream) {
  k_main<<<256, 512, 0, stream>>>((const u16*)d_in[0], (const u16*)d_in[1], (const u16*)d_in[2], (const u16*)d_in[3], (const u16*)d_in[4],
                                  (const u16*)d_in[5], (const u16*)d_in[6], (const u16*)d_in[7], (const u16*)d_in[8], (u16*)d_out, (char*)d_ws);
}
```

```cpp
#include <hip/hip_runtime.h>
#include <hip/hip_bf16.h>
#include <stdint.h>
#include <utility>

#define DI __device__ __forceinline__
typedef __bf16 bf16x8 __attribute__((ext_vector_type(8)));
typedef __bf16 bf16x2 __attribute__((ext_vector_type(2)));
typedef float f32x4 __attribute__((ext_vector_type(4)));
typedef float f32x2 __attribute__((ext_vector_type(2)));
typedef unsigned u32x4 __attribute__((ext_vector_type(4)));
typedef unsigned u32x2 __attribute__((ext_vector_type(2)));
typedef unsigned short u16;
#define GAS __attribute__((address_space(1)))
typedef GAS unsigned long long gu64;
typedef GAS unsigned gu32;
#define RLX_AGENT __ATOMIC_RELAXED, __HIP_MEMORY_SCOPE_AGENT

constexpr int NCH = 512, HW = 4096, NEMB = 768;
constexpr float GN_EPS = 1e-5f;
constexpr unsigned SPIN_CAP = 1u << 20;

constexpr size_t U_BYTES = (size_t)8 * 16 * 49152;
constexpr size_t V_BYTES = (size_t)2 * 16 * 4 * 131072;
constexpr size_t OFF_U1 = 0, OFF_U2 = U_BYTES;
constexpr size_t OFF_V1 = 2 * U_BYTES, OFF_V2 = OFF_V1 + V_BYTES;
constexpr size_t OFF_EPOCH = OFF_V2 + V_BYTES;
constexpr size_t OFF_GRAN = OFF_EPOCH + 256;
constexpr size_t OFF_LINES = OFF_GRAN + 8192;
constexpr int W_HALO = 0, W_V1 = 4, W_V2 = 12, W_U1 = 20, W_U2 = 24;
constexpr size_t OFF_EMBP = OFF_LINES + 256 * 128;
constexpr size_t OFF_HALO = OFF_EMBP + 8 * 32 * 512;
constexpr int HALO_STRIDE = 18432;
constexpr size_t OFF_DIAG = OFF_HALO + 256 * HALO_STRIDE;
#define STAMP(i)
#define STAMPR(i)

DI float bf_lo(unsigned v) { return __uint_as_float(v << 16); }
DI float bf_hi(unsigned v) { return __uint_as_float(v & 0xffff0000u); }
DI unsigned pack_bf16(float lo, float hi) {
  f32x2 v = {lo, hi};
  bf16x2 b = __builtin_convertvector(v, bf16x2);
  return __builtin_bit_cast(unsigned, b);
}
DI float silu_f(float y) { return y * __builtin_amdgcn_rcpf(1.f + __builtin_amdgcn_exp2f(y * -1.44269504088896341f)); }
DI f32x2 pk_sub(f32x2 a, f32x2 b) { return __builtin_elementwise_fma(b, f32x2{-1.f, -1.f}, a); }
DI f32x2 lo2(const f32x4& v) { return __builtin_shufflevector(v, v, 0, 1); }
DI f32x2 hi2(const f32x4& v) { return __builtin_shufflevector(v, v, 2, 3); }
DI f32x4 cat2(f32x2 a, f32x2 b) { return __builtin_shufflevector(a, b, 0, 1, 2, 3); }
DI f32x4 sub4(const f32x4& a, const f32x4& b) { return cat2(pk_sub(lo2(a), lo2(b)), pk_sub(hi2(a), hi2(b))); }
DI f32x2 silu_aff2(f32x2 h, f32x2 sa, f32x2 sb, f32x2 sc, f32x2 sd) {
  const f32x2 y = __builtin_elementwise_fma(h, sa, sb), t = __builtin_elementwise_fma(h, sc, sd);
  const f32x2 d = f32x2{__builtin_amdgcn_exp2f(t[0]), __builtin_amdgcn_exp2f(t[1])} + 1.f;
  return y * f32x2{__builtin_amdgcn_rcpf(d[0]), __builtin_amdgcn_rcpf(d[1])};
}
template <int CTRL, int RM = 0xf> DI float dpp0(float v) {
  return __builtin_bit_cast(float, __builtin_amdgcn_update_dpp(0, __builtin_bit_cast(int, v), CTRL, RM, 0xf, true));
}
DI float dpp_prev(float v) { return dpp0<0x111>(v); }
DI float dpp_next(float v) { return dpp0<0x101>(v); }
DI float wave_sum(float v) {
  v += dpp0<0x111>(v); v += dpp0<0x112>(v); v += dpp0<0x114>(v); v += dpp0<0x118>(v);
  v += dpp0<0x142, 0xa>(v);
  v += dpp0<0x143, 0xc>(v);
  return __builtin_bit_cast(float, __builtin_amdgcn_readlane(__builtin_bit_cast(int, v), 63));
}
DI int vpos(int o, int xt) { return (((o & 1) << 1) | (o >> 1)) ^ ((xt >> 2) & 1); }
DI void bar() { asm volatile("s_barrier" ::: "memory"); }
DI void lds_bar() { asm volatile("s_waitcnt lgkmcnt(0)\n\ts_barrier" ::: "memory"); }
template <int N> DI void wait_vm() { asm volatile("s_waitcnt vmcnt(%0)" :: "i"(N) : "memory"); }

DI void gn_combine(float s, float q, bool on, float n, float& mean, float& rstd) {
  const float tot = wave_sum(on ? s : 0.f);
  mean = tot * (1.f / (32.f * n));
  const float d = s * (1.f / n) - mean;
  const float m2 = wave_sum(on ? (q + n * d * d) : 0.f);
  rstd = __builtin_amdgcn_rsqf(m2 * (1.f / (32.f * n)) + GN_EPS);
}

constexpr int VSLOT = 20480, NVSLOT = 4;
constexpr int USLOT = 24576, NUSLOT = 3;
constexpr int LDS_U = NVSLOT * VSLOT, LDS_MISC = LDS_U + NUSLOT * USLOT, LDS_TOTAL = 163840;
constexpr int NUNIT = 32;
constexpr int M_EMB = LDS_MISC, M_GAM = LDS_MISC + 256, M_BET = LDS_MISC + 512, M_RED = LDS_MISC + 768, M_ZERO = LDS_MISC + 960, M_RAW = LDS_MISC + 1024, M_COL = LDS_MISC + 3584, M_DUMP = LDS_MISC + 7680;
constexpr int X_HAND = 65536;

struct Tile { int bid, b, nt, ty, tx, y0; };
DI int tile_bid(int b, int ty, int tx, int nt) { return (((ty & 3) * 8 + tx * 4 + (nt & 3)) << 3) | (b * 4 + (ty >> 2) * 2 + (nt >> 2)); }
DI int chunk_of(int j, int nt) { return j < 2 ? 2 * nt + j : (j - 2 < 2 * nt ? j - 2 : j); }

struct Loader {
  __amdgpu_buffer_rsrc_t rv, ru;
  unsigned vfirst, vmid, vlast;
  unsigned sv, su;
  unsigned mv, mu;
  int nt;
};
DI void dma1(unsigned m0, unsigned voff, __amdgpu_buffer_rsrc_t r, unsigned soff) {
  asm volatile("s_mov_b32 m0, %0\n\ts_nop 0\n\tbuffer_load_dwordx4 %1, %2, %3 offen lds" :: "s"(m0), "v"(voff), "s"(r), "s"(soff) : "memory");
}
DI void dma1_dev(unsigned m0, unsigned voff, __amdgpu_buffer_rsrc_t r, unsigned soff) {
  asm volatile("s_mov_b32 m0, %0\n\ts_nop 0\n\tbuffer_load_dwordx4 %1, %2, %3 offen sc1 lds" :: "s"(m0), "v"(voff), "s"(r), "s"(soff) : "memory");
}
DI void ld_u(const Loader& L, int k) {
  const unsigned mu = L.mu + (unsigned)(k % NUSLOT) * USLOT, su = L.su + (unsigned)(chunk_of(k >> 1, L.nt) * 2 + (k & 1)) * USLOT;
  asm volatile("s_mov_b32 m0, %0\n\ts_nop 0\n\t"
               "buffer_load_dwordx4 %1, %2, %3 offen lds\n\t"
               "buffer_load_dwordx4 %1, %2, %3 offen offset:1024 lds\n\t"
               "buffer_load_dwordx4 %1, %2, %3 offen offset:2048 lds\n\t"
               "buffer_load_dwordx4 %1, %2, %3 offen offset:3072 lds"
               :: "s"(mu), "v"(L.vmid), "s"(L.ru), "s"(su) : "memory");
  asm volatile("s_mov_b32 m0, %0\n\ts_nop 0\n\t"
               "buffer_load_dwordx4 %1, %2, %3 offen lds\n\t"
               "buffer_load_dwordx4 %1, %2, %3 offen offset:1024 lds"
               :: "s"(mu + 4096), "v"(L.vmid), "s"(L.ru), "s"(su + 4096) : "memory");
}
DI void ld_v(const Loader& L, int k) {
  const unsigned mv = L.mv + (unsigned)(k & 3) * VSLOT, sv = L.sv + (unsigned)(chunk_of(k >> 1, L.nt) * 2 + (k & 1)) * 262144u;
  dma1_dev(mv, L.vfirst, L.rv, sv);
  dma1_dev(mv + 1024, L.vmid, L.rv, sv + 2048);
  dma1_dev(mv + 2048, L.vmid, L.rv, sv + 4096);
  dma1_dev(mv + 3072, L.vmid, L.rv, sv + 6144);
  dma1_dev(mv + 4096, L.vlast, L.rv, sv + 8192);
}

struct Comp {
  f32x4 acc[2][8][2];
  bf16x8 A[10];
  bf16x8 B[2][6];
  unsigned abase, bbase;
  unsigned acur, anxt, bnxt;
};
struct MD { int r, dy, o; };
constexpr MD mdecode(int m) {
  int n = 0;
  for (int r = 0; r < 10; ++r)
    for (int dy = 0; dy < 3; ++dy) {
      if (r - dy < 0 || r - dy > 7) continue;
      for (int o = 0; o < 2; ++o) { if (n == m) return MD{r, dy, o}; ++n; }
    }
  return MD{-1, -1, -1};
}
template <int OFF> DI void lds_rd(bf16x8& dst, unsigned addr) { asm volatile("ds_read_b128 %0, %1 offset:%2" : "=v"(dst) : "v"(addr), "i"(OFF)); }
template <int N> DI void wait_lgkm() { asm volatile("s_waitcnt lgkmcnt(%0)" :: "i"(N) : "memory"); }

constexpr int first_wait(int m) {
  switch (m) { case 2: return 3; case 6: return 3; case 12: return 4; case 18: return 5; case 24: return 6; case 30: return 7;
               case 36: return 8; case 42: return 9; case 46: return 10; default: return -1; }
}
template <int MODE, int E, int M> DI void mstep(Comp& s) {
  constexpr MD d = mdecode(M);
  if constexpr (first_wait(M) >= 0) wait_lgkm<first_wait(M)>();
  if constexpr (MODE == 1) asm volatile("v_mfma_f32_16x16x32_bf16 %0, %1, %2, %0" : "+v"(s.acc[E][d.r - d.dy][d.o]) : "v"(s.B[E][d.dy * 2 + d.o]), "v"(s.A[d.r]));
  else                     asm volatile("v_mfma_f32_16x16x32_bf16 %0, %1, %2, %0" : "+v"(s.acc[E][d.r - d.dy][d.o]) : "v"(s.A[d.r]), "v"(s.B[E][d.dy * 2 + d.o]));
  if constexpr (M % 3 == 0) {
    constexpr int j = M / 3;
    if constexpr (j < 6) lds_rd<(4 + j) * 1024>(s.A[4 + j], s.acur);
    else if constexpr (j == 6) lds_rd<0>(s.B[E ^ 1][0], s.bnxt);
    else if constexpr (j == 7) lds_rd<1024>(s.B[E ^ 1][1], s.bnxt);
    else if constexpr (j == 8) lds_rd<0>(s.A[0], s.anxt);
    else if constexpr (j == 9) lds_rd<4096>(s.B[E ^ 1][2], s.bnxt);
    else if constexpr (j == 10) lds_rd<4096 + 1024>(s.B[E ^ 1][3], s.bnxt);
    else if constexpr (j == 11) lds_rd<8192>(s.B[E ^ 1][4], s.bnxt);
    else if constexpr (j == 12) lds_rd<8192 + 1024>(s.B[E ^ 1][5], s.bnxt);
    else lds_rd<(j - 12) * 1024>(s.A[j - 12], s.anxt);
  }
}
template <int MODE, int E, int... Ms> DI void msteps(Comp& s, std::integer_sequence<int, Ms...>) { (mstep<MODE, E, Ms>(s), ...); }

template <int MODE, int E> DI void unit(Comp& s, unsigned& voff, unsigned& uoff) {
  bar();
  s.acur = s.anxt;
  voff = (voff + VSLOT) == NVSLOT * VSLOT ? 0u : voff + VSLOT;
  s.anxt = s.abase + voff;
  uoff = (uoff + USLOT) == NUSLOT * USLOT ? 0u : uoff + USLOT;
  s.bnxt = s.bbase + uoff;
  msteps<MODE, E>(s, std::make_integer_sequence<int, 48>{});
  wait_lgkm<3>();
}

struct Affine { f32x2 sa[4], sb[4], sc[4], sd[4]; };
DI void row_transform(const f32x4 (&ye)[2], const f32x4 (&yo)[2], const f32x4 (&hl)[2], const f32x4 (&hr)[2], const Affine& af, u32x4 (&o)[4]) {
#pragma unroll
  for (int q = 0; q < 4; ++q) {
    const f32x2 he = (q & 1) ? hi2(ye[q >> 1]) : lo2(ye[q >> 1]), ho2 = (q & 1) ? hi2(yo[q >> 1]) : lo2(yo[q >> 1]);
    const f32x2 d1 = silu_aff2(he, af.sa[q], af.sb[q], af.sc[q], af.sd[q]);
    const f32x2 d2 = silu_aff2(ho2, af.sa[q], af.sb[q], af.sc[q], af.sd[q]);
    const f32x2 d2l = pk_sub(d2, (q & 1) ? hi2(hl[q >> 1]) : lo2(hl[q >> 1])), d1r = pk_sub(d1, (q & 1) ? hi2(hr[q >> 1]) : lo2(hr[q >> 1]));
    const f32x2 t1 = d1 + d2, t2 = pk_sub(d2, d1);
    o[0][q] = pack_bf16(dpp_prev(d2[0]) - d2l[0], dpp_prev(d2[1]) - d2l[1]);
    o[1][q] = pack_bf16(t1[0], t1[1]);
    o[2][q] = pack_bf16(t2[0], t2[1]);
    o[3][q] = pack_bf16(d1r[0] - dpp_next(d1[0]), d1r[1] - dpp_next(d1[1]));
  }
}
DI void halo_ops(const char* lds, const float* c, int tx, int i16, int kg, f32x4 (&hl)[2], f32x4 (&hr)[2]) {
  const float* z = (const float*)(lds + M_ZERO);
  const float* pl = (tx == 1 && i16 == 0) ? c + 8 * kg : z;
  const float* pr = (tx == 0 && i16 == 15) ? c + 8 * kg : z;
  hl[0] = *(const f32x4*)pl; hl[1] = *(const f32x4*)(pl + 4);
  hr[0] = *(const f32x4*)pr; hr[1] = *(const f32x4*)(pr + 4);
}

template <int P> DI void exchange(const Comp& s, char* lds, int wid, int lane, f32x4 (&ye)[4][2], f32x4 (&yo)[4][2]) {
  char* xb = lds + wid * 16384 + lane * 16;
  const char* rb = lds + (wid ^ 1) * 16384 + lane * 16;
#pragma unroll
  for (int r = 0; r < 4; ++r)
#pragma unroll
    for (int o = 0; o < 2; ++o) {
      if constexpr (P == 0) {
        *(f32x4*)(xb + (r * 4 + o * 2) * 1024) = s.acc[0][4 + r][o] + s.acc[1][4 + r][o];
        *(f32x4*)(xb + (r * 4 + o * 2 + 1) * 1024) = s.acc[1][4 + r][o];
      } else {
        *(f32x4*)(xb + (r * 4 + o * 2) * 1024) = s.acc[0][r][o];
        *(f32x4*)(xb + (r * 4 + o * 2 + 1) * 1024) = s.acc[0][r][o] + s.acc[1][r][o];
      }
    }
  lds_bar();
  f32x4 r0[4][2], r1[4][2];
#pragma unroll
  for (int r = 0; r < 4; ++r)
#pragma unroll
    for (int o = 0; o < 2; ++o) { r0[r][o] = *(const f32x4*)(rb + (r * 4 + o * 2) * 1024); r1[r][o] = *(const f32x4*)(rb + (r * 4 + o * 2 + 1) * 1024); }
#pragma unroll
  for (int r = 0; r < 4; ++r)
#pragma unroll
    for (int o = 0; o < 2; ++o) {
      if constexpr (P == 0) { ye[r][o] = s.acc[0][r][o] + s.acc[1][r][o] + r0[r][o]; yo[r][o] = sub4(s.acc[1][r][o], r1[r][o]); }
      else                  { ye[r][o] = r0[r][o] + s.acc[0][4 + r][o]; yo[r][o] = sub4(r1[r][o], s.acc[0][4 + r][o] + s.acc[1][4 + r][o]); }
    }
}

DI void load_affine(Affine& af, const char* lds, int ho, int kg) {
  const float* red = (const float*)(lds + M_RED);
  const float mean = red[16], rstd = red[17];
  const f32x4 g0 = *(const f32x4*)(lds + M_GAM + (32 * ho + 8 * kg) * 4), g1 = *(const f32x4*)(lds + M_GAM + (32 * ho + 8 * kg) * 4 + 16);
  const f32x4 b0 = *(const f32x4*)(lds + M_BET + (32 * ho + 8 * kg) * 4), b1 = *(const f32x4*)(lds + M_BET + (32 * ho + 8 * kg) * 4 + 16);
#pragma unroll
  for (int q = 0; q < 4; ++q) {
    const f32x2 g = q == 0 ? lo2(g0) : q == 1 ? hi2(g0) : q == 2 ? lo2(g1) : hi2(g1);
    const f32x2 bb = q == 0 ? lo2(b0) : q == 1 ? hi2(b0) : q == 2 ? lo2(b1) : hi2(b1);
    af.sa[q] = g * rstd; af.sb[q] = bb - af.sa[q] * mean;
    af.sc[q] = af.sa[q] * -1.44269504088896341f; af.sd[q] = af.sb[q] * -1.44269504088896341f;
  }
}
DI void set_tag(char* ws, int bid, int word, unsigned ep, int lane) {
  if (lane == 0) __hip_atomic_store((gu32*)(ws + OFF_LINES) + bid * 32 + word, ep, RLX_AGENT);
}
template <int NQ> DI void wait_tags(char* ws, int who, int w0, bool need, unsigned ep) {
  const __amdgpu_buffer_rsrc_t rl = __builtin_amdgcn_make_buffer_rsrc((void*)(ws + OFF_LINES), (short)0, 256 * 128, 0x00020000);
  for (unsigned spins = 0;; ++spins) {
    bool ok = true;
#pragma unroll
    for (int q = 0; q < NQ; ++q) {
      const u32x4 v = __builtin_amdgcn_raw_buffer_load_b128(rl, who * 128 + w0 * 4 + q * 16, 0, 16);
      ok = ok && v[0] == ep && v[1] == ep && v[2] == ep && v[3] == ep;
    }
    if (__all(!need || ok)) break;
    if (spins > SPIN_CAP) break;
    __builtin_amdgcn_s_sleep(2);
    asm volatile("" ::: "memory");
  }
  asm volatile("" ::: "memory");
}
DI unsigned lds_key(unsigned ep, int idx) { return (ep * 16u + (unsigned)idx) * 0x9E3779B1u + 0x3C6EF372u; }
DI void lds_flag_set(char* lds, int idx, unsigned ep) {
  const unsigned key = lds_key(ep, idx);
  __hip_atomic_store((__attribute__((address_space(3))) unsigned*)(lds + M_RED + 96 + idx * 4), key, __ATOMIC_RELAXED, __HIP_MEMORY_SCOPE_WORKGROUP);
}
DI void lds_flag_wait(char* lds, int idx, unsigned ep) {
  const unsigned key = lds_key(ep, idx);
  for (unsigned spins = 0; spins < (SPIN_CAP << 4); ++spins) {
    const unsigned v = __hip_atomic_load((__attribute__((address_space(3))) unsigned*)(lds + M_RED + 96 + idx * 4), __ATOMIC_RELAXED, __HIP_MEMORY_SCOPE_WORKGROUP);
    if (__builtin_amdgcn_readfirstlane(v) == key) break;
    __builtin_amdgcn_s_sleep(1);
  }
  asm volatile("" ::: "memory");
}
DI int gran_slot(const Tile& T) { return T.ty * 4 + T.tx * 2 + (T.nt & 1); }
DI void group_stats(char* ws, int conv, const Tile& T, float tot, float m2, unsigned ep, int lane) {
  gu64* gran = (gu64*)(ws + OFF_GRAN) + (conv * 8 + T.b * 4 + (T.nt >> 1)) * 64;
  if (lane < 2) {
    const float val = lane == 0 ? tot : m2;
    __hip_atomic_store(gran + gran_slot(T) * 2 + lane, ((unsigned long long)ep << 32) | (unsigned long long)__float_as_uint(val), RLX_AGENT);
  }
}
DI void group_stats_wait(char* ws, int conv, const Tile& T, float tot, float m2, unsigned ep, float* red, int lane) {
  gu64* gran = (gu64*)(ws + OFF_GRAN) + (conv * 8 + T.b * 4 + (T.nt >> 1)) * 64;
  const bool own = (lane >> 1) == gran_slot(T);
  unsigned long long g;
  for (unsigned spins = 0;; ++spins) {
    g = __hip_atomic_load(gran + lane, RLX_AGENT);
    if (__all(own || (unsigned)(g >> 32) == ep)) break;
    if (spins > SPIN_CAP) break;
    __builtin_amdgcn_s_sleep(1);
  }
  const float val = own ? ((lane & 1) ? m2 : tot) : __uint_as_float((unsigned)g);
  const float qk = dpp0<0x101>(val);
  float mean, rstd;
  gn_combine(val, qk, !(lane & 1), 16384.f, mean, rstd);
  if (lane == 0) { red[16] = mean; red[17] = rstd; }
}

DI void put_row(char* lds, __amdgpu_buffer_rsrc_t rv, const Tile& T, int ho, int rho, bool publish, int i16, int kg, const u32x4 (&o)[4]) {
  const int pos = i16 * 64 + vpos(kg, i16) * 16;
  char* d = lds + 2 * ho * VSLOT + rho * 1024 + pos;
#pragma unroll
  for (int xi = 0; xi < 4; ++xi) *(u32x4*)(d + (xi & 1) * VSLOT + (xi >> 1) * 10240) = o[xi];
  if (publish) {
    const int so = (T.b * 16 + 2 * T.nt + ho) * 524288 + (T.y0 - 1 + rho) * 2048 + T.tx * 1024;
#pragma unroll
    for (int xi = 0; xi < 4; ++xi)
      __builtin_amdgcn_raw_buffer_store_b128(o[xi], rv, pos + so + (xi & 1) * 262144 + (xi >> 1) * 131072, 0, 16);
  }
}

template <int NR> struct XRows { unsigned v[NR][8]; unsigned short col[2]; u32x4 gv, bv; unsigned short gc, bc; };
DI int clamp_y(int y) { return min(max(y, 0), 63); }
template <int NR> DI void rows_load(XRows<NR>& X, const int (&rho)[NR], int rc0, int rc1, const u16* __restrict__ x, const u16* __restrict__ gam,
                                    const u16* __restrict__ bet, const Tile& T, int ho, int lane) {
  const int i16 = lane & 15, kg = lane >> 4, chl = lane & 31;
  const u16* xb = x + (size_t)(T.b * NCH + T.nt * 64 + 32 * ho) * HW;
#pragma unroll
  for (int k = 0; k < NR; ++k)
#pragma unroll
    for (int j = 0; j < 8; ++j) X.v[k][j] = *(const unsigned*)(xb + (size_t)(8 * kg + j) * HW + clamp_y(T.y0 - 1 + rho[k]) * 64 + 32 * T.tx + 2 * i16);
  const int xc = T.tx == 0 ? 32 : 31;
  X.col[0] = xb[(size_t)chl * HW + clamp_y(T.y0 - 1 + rc0) * 64 + xc];
  X.col[1] = xb[(size_t)chl * HW + clamp_y(T.y0 - 1 + rc1) * 64 + xc];
  const int ch = T.nt * 64 + 32 * ho;
  X.gv = *(const u32x4*)(gam + ch + 8 * kg); X.bv = *(const u32x4*)(bet + ch + 8 * kg);
  X.gc = gam[ch + chl]; X.bc = bet[ch + chl];
}
template <int NR, int NI> DI void rows_stats(const XRows<NR>& X, float* red, int wid, int lane) {
  const float piv = __builtin_bit_cast(float, __builtin_amdgcn_readfirstlane(X.v[0][0] << 16));
  f32x2 sv = {0.f, 0.f}, qv = {0.f, 0.f};
#pragma unroll
  for (int k = 0; k < NI; ++k)
#pragma unroll
    for (int j = 0; j < 8; ++j) {
      const f32x2 d = f32x2{bf_lo(X.v[k][j]), bf_hi(X.v[k][j])} - piv;
      sv += d; qv = __builtin_elementwise_fma(d, d, qv);
    }
  const float s = wave_sum(sv[0] + sv[1]), q = wave_sum(qv[0] + qv[1]);
  if (lane == 0) { red[wid] = s + piv * (float)(NI * 1024); red[8 + wid] = q - s * s * (1.f / (float)(NI * 1024)); }
}
template <int NR> DI void rows_emit(const XRows<NR>& X, const int (&rho)[NR], int k0, int k1, char* lds, char* ws, const Tile& T, int wid, int ho, int lane) {
  const int i16 = lane & 15, kg = lane >> 4;
  const float* red = (const float*)(lds + M_RED);
  const float mean = red[16], rstd = red[17];
  float sa[8], sb[8];
#pragma unroll
  for (int j = 0; j < 8; ++j) {
    sa[j] = rstd * ((j & 1) ? bf_hi(X.gv[j >> 1]) : bf_lo(X.gv[j >> 1]));
    sb[j] = ((j & 1) ? bf_hi(X.bv[j >> 1]) : bf_lo(X.bv[j >> 1])) - mean * sa[j];
  }
  const float* col = (const float*)(lds + M_COL + wid * 512);
  const __amdgpu_buffer_rsrc_t rv = __builtin_amdgcn_make_buffer_rsrc((void*)(ws + OFF_V1), (short)0, (int)V_BYTES, 0x00020000);
#pragma unroll
  for (int k = 0; k < NR; ++k) {
    if (k < k0 || k >= k1) continue;
    f32x4 hl[2], hr[2];
    halo_ops(lds, col + k * 32, T.tx, i16, kg, hl, hr);
    float t[4][8];
#pragma unroll
    for (int j = 0; j < 8; ++j) {
      const f32x2 a2 = {sa[j], sa[j]}, b2 = {sb[j], sb[j]};
      const f32x2 d = silu_aff2(f32x2{bf_lo(X.v[k][j]), bf_hi(X.v[k][j])}, a2, b2, a2 * -1.44269504088896341f, b2 * -1.44269504088896341f);
      t[0][j] = dpp_prev(d[1]) - (d[1] - hl[j >> 2][j & 3]);
      t[1][j] = d[0] + d[1];
      t[2][j] = d[1] - d[0];
      t[3][j] = (d[0] - hr[j >> 2][j & 3]) - dpp_next(d[0]);
    }
    u32x4 o[4];
#pragma unroll
    for (int xi = 0; xi < 4; ++xi)
      o[xi] = u32x4{pack_bf16(t[xi][0], t[xi][1]), pack_bf16(t[xi][2], t[xi][3]), pack_bf16(t[xi][4], t[xi][5]), pack_bf16(t[xi][6], t[xi][7])};
    const int y = T.y0 - 1 + rho[k];
    const bool halo_row = NR == 2 && k == 1;
    if (halo_row && (y < 0 || y > 63)) { o[0] = o[1] = o[2] = o[3] = u32x4{0u, 0u, 0u, 0u}; }
    put_row(lds, rv, T, ho, rho[k], !halo_row, i16, kg, o);
  }
}
template <int NR> DI void rows_column(const XRows<NR>& X, char* lds, int wid, int lane) {
  const int chl = lane & 31, half = lane >> 5;
  const float* red = (const float*)(lds + M_RED);
  const float mean = red[16], rstd = red[17];
  float* col = (float*)(lds + M_COL + wid * 512);
  const float sa = rstd * __uint_as_float((unsigned)X.gc << 16), sb = __uint_as_float((unsigned)X.bc << 16) - mean * sa;
  col[lane] = silu_f(__uint_as_float((unsigned)X.col[0] << 16) * sa + sb);
  if constexpr (NR >= 3) {
    const float v2 = silu_f(__uint_as_float((unsigned)X.col[1] << 16) * sa + sb);
    if (half == 0) col[64 + chl] = v2;
  }
}

DI int u_row(int conv, int of, int i) { return conv == 0 ? (32 * (of >> 1) + 8 * (i >> 2) + 4 * (of & 1) + (i & 3)) : (16 * of + i); }
DI void u_load(u32x4 (&d)[9], const u16* __restrict__ w, int conv, const Tile& T, int uidx, int of, int lane) {
  const int i = lane & 15, kg = lane >> 4;
  const u16* src = w + ((size_t)(T.nt * 64 + u_row(conv, of, i)) * 512 + 32 * (uidx >> 1) + 8 * kg) * 9;
  asm volatile("global_load_dwordx4 %0, %9, off\n\tglobal_load_dwordx4 %1, %9, off offset:16\n\tglobal_load_dwordx4 %2, %9, off offset:32\n\t"
               "global_load_dwordx4 %3, %9, off offset:48\n\tglobal_load_dwordx4 %4, %9, off offset:64\n\tglobal_load_dwordx4 %5, %9, off offset:80\n\t"
               "global_load_dwordx4 %6, %9, off offset:96\n\tglobal_load_dwordx4 %7, %9, off offset:112\n\tglobal_load_dwordx4 %8, %9, off offset:128"
               : "=&v"(d[0]), "=&v"(d[1]), "=&v"(d[2]), "=&v"(d[3]), "=&v"(d[4]), "=&v"(d[5]), "=&v"(d[6]), "=&v"(d[7]), "=&v"(d[8])
               : "v"(src) : "memory");
}
DI void u_landed(u32x4 (&d)[9]) {
  asm volatile("" : "+v"(d[0]), "+v"(d[1]), "+v"(d[2]), "+v"(d[3]), "+v"(d[4]), "+v"(d[5]), "+v"(d[6]), "+v"(d[7]), "+v"(d[8]) :: "memory");
}
DI void u_stage(unsigned ldsdst, const u16* __restrict__ w, int conv, const Tile& T, int uidx, int of, int lane) {
  const __amdgpu_buffer_rsrc_t rw = __builtin_amdgcn_make_buffer_rsrc((void*)w, (short)0, 512 * 512 * 9 * 2, 0x00020000);
#pragma unroll
  for (int k = 0; k < 9; ++k) {
    const int t = k * 64 + lane, row = t / 36, c16 = t - row * 36;
    dma1(ldsdst + k * 1024, (unsigned)(((T.nt * 64 + u_row(conv, of, row)) * 512 + 32 * (uidx >> 1)) * 18 + c16 * 16), rw, 0u);
  }
}
DI void u_fetch(u32x4 (&d)[9], const char* stage, int lane) {
#pragma unroll
  for (int k = 0; k < 9; ++k) d[k] = *(const u32x4*)(stage + (lane & 15) * 576 + (lane >> 4) * 144 + k * 16);
}
DI void u_emit(const u32x4 (&d)[9], char* ws, int conv, const Tile& T, int uidx, int of, int lane) {
  const __amdgpu_buffer_rsrc_t ru = __builtin_amdgcn_make_buffer_rsrc((void*)(ws + (conv == 0 ? OFF_U1 : OFF_U2)), (short)0, (int)U_BYTES, 0x00020000);
  const int dst = T.nt * 786432 + uidx * USLOT + of * 1024 + lane * 16;
  const float sg = (uidx & 1) ? 1.f : -1.f;
#pragma unroll
  for (int dy = 0; dy < 3; ++dy) {
    float ua[8], ub[8];
#pragma unroll
    for (int j = 0; j < 8; ++j) {
      float g[3];
#pragma unroll
      for (int dx = 0; dx < 3; ++dx) {
        const int e = 9 * j + 3 * dy + dx;
        const unsigned dw = d[e >> 3][(e >> 1) & 3];
        g[dx] = (e & 1) ? bf_hi(dw) : bf_lo(dw);
      }
      const float mid = 0.5f * (g[0] + sg * g[1] + g[2]);
      ua[j] = (uidx & 1) ? mid : g[0];
      ub[j] = (uidx & 1) ? g[2] : mid;
    }
    const u32x4 oa = {pack_bf16(ua[0], ua[1]), pack_bf16(ua[2], ua[3]), pack_bf16(ua[4], ua[5]), pack_bf16(ua[6], ua[7])};
    const u32x4 ob = {pack_bf16(ub[0], ub[1]), pack_bf16(ub[2], ub[3]), pack_bf16(ub[4], ub[5]), pack_bf16(ub[6], ub[7])};
    __builtin_amdgcn_raw_buffer_store_b128(oa, ru, dst + dy * 4096, 0, 16);
    __builtin_amdgcn_raw_buffer_store_b128(ob, ru, dst + 12288 + dy * 4096, 0, 16);
  }
}

template <int P> DI void epi1_mfma(const Comp& s, char* lds, char* ws, const Tile& T, unsigned ep, int wid, int lane) {
  constexpr int MODE = 1; (void)MODE;
  const int i16 = lane & 15, kg = lane >> 4, ho = wid >> 1;
  f32x4 ye[4][2], yo[4][2];
  exchange<P>(s, lds, wid, lane, ye, yo);
  if (wid == 0) { STAMP(12); }
  {
    const f32x4 e0 = *(const f32x4*)(lds + M_EMB + (32 * ho + 8 * kg) * 4), e1 = *(const f32x4*)(lds + M_EMB + (32 * ho + 8 * kg) * 4 + 16);
#pragma unroll
    for (int r = 0; r < 4; ++r) { ye[r][0] += e0; ye[r][1] += e1; yo[r][0] += e0; yo[r][1] += e1; }
  }
  const __amdgpu_buffer_rsrc_t rh = __builtin_amdgcn_make_buffer_rsrc((void*)(ws + OFF_HALO), (short)0, 256 * HALO_STRIDE, 0x00020000);
  float* red = (float*)(lds + M_RED);
  const int hb = T.bid * HALO_STRIDE;
  constexpr int RE = P == 0 ? 0 : 3;
#pragma unroll
  for (int f = 0; f < 2; ++f) {
    __builtin_amdgcn_raw_buffer_store_b128(__builtin_bit_cast(u32x4, ye[RE][f]), rh, hb + P * 8192 + ((ho * 2 + f) * 2) * 1024 + lane * 16, 0, 16);
    __builtin_amdgcn_raw_buffer_store_b128(__builtin_bit_cast(u32x4, yo[RE][f]), rh, hb + P * 8192 + ((ho * 2 + f) * 2 + 1) * 1024 + lane * 16, 0, 16);
  }
  if (i16 == (T.tx == 0 ? 15 : 0)) {
#pragma unroll
    for (int r = 0; r < 4; ++r)
#pragma unroll
      for (int f = 0; f < 2; ++f)
        __builtin_amdgcn_raw_buffer_store_b128(__builtin_bit_cast(u32x4, T.tx == 0 ? yo[r][f] : ye[r][f]), rh,
                                               hb + 16384 + (4 * P + r) * 256 + (32 * ho + 8 * kg + 4 * f) * 4, 0, 16);
  }
  {
    const float piv = __builtin_bit_cast(float, __builtin_amdgcn_readfirstlane(__builtin_bit_cast(int, ye[0][0][0])));
    f32x4 sv = {0.f, 0.f, 0.f, 0.f}, qv = {0.f, 0.f, 0.f, 0.f};
#pragma unroll
    for (int r = 0; r < 4; ++r)
#pragma unroll
      for (int f = 0; f < 2; ++f) {
        const f32x4 d0 = ye[r][f] - piv, d1 = yo[r][f] - piv;
        sv += d0 + d1;
        qv = __builtin_elementwise_fma(d0, d0, qv);
        qv = __builtin_elementwise_fma(d1, d1, qv);
      }
    const float sum = wave_sum((sv[0] + sv[1]) + (sv[2] + sv[3])), q = wave_sum((qv[0] + qv[1]) + (qv[2] + qv[3]));
    if (lane == 0) { red[wid] = sum + piv * 4096.f; red[8 + wid] = q - sum * sum * (1.f / 4096.f); }
  }
  constexpr int RH = P == 0 ? 3 : 0;
  char* hd = lds + X_HAND + wid * 4096 + lane * 16;
#pragma unroll
  for (int f = 0; f < 2; ++f) { *(f32x4*)(hd + (f * 2) * 1024) = ye[RH][f]; *(f32x4*)(hd + (f * 2 + 1) * 1024) = yo[RH][f]; }
  lds_bar();
  if (wid == 0) { STAMP(13); }
  wait_vm<0>();
  set_tag(ws, T.bid, W_HALO + wid, ep, lane);
  const int chl = lane & 31, half = lane >> 5;
  const float gam_c = ((const float*)(lds + M_GAM))[32 * ho + chl], bet_c = ((const float*)(lds + M_BET))[32 * ho + chl];
  lds_bar();
  if (wid == 0) { STAMP(11); }
  Affine af;
  load_affine(af, lds, ho, kg);
  float* col = (float*)(lds + M_COL + wid * 512);
  {
    const float* raw = (const float*)(lds + M_RAW);
    const float sa = red[17] * gam_c, sb = bet_c - red[16] * sa;
    col[lane] = silu_f(raw[(5 * P + 1 + half) * 64 + 32 * ho + chl] * sa + sb);
    const float v2 = silu_f(raw[(5 * P + 3) * 64 + 32 * ho + chl] * sa + sb);
    if (half == 0) col[64 + chl] = v2;
  }
  if (wid == 0) { STAMP(18); }
  const __amdgpu_buffer_rsrc_t rv = __builtin_amdgcn_make_buffer_rsrc((void*)(ws + OFF_V2), (short)0, (int)V_BYTES, 0x00020000);
#pragma unroll
  for (int k = 0; k < 3; ++k) {
    constexpr int R0 = P == 0 ? 0 : 1;
    f32x4 hl[2], hr[2];
    halo_ops(lds, col + k * 32, T.tx, i16, kg, hl, hr);
    u32x4 o[4];
    row_transform(ye[R0 + k], yo[R0 + k], hl, hr, af, o);
    put_row(lds, rv, T, ho, 4 * P + R0 + k + 1, true, i16, kg, o);
  }
  if (wid == 0) { STAMP(19); }
}

DI void epi1_loader(const Loader& L, const u16* __restrict__ w2, int uprod, int uidx, char* lds, char* ws, const Tile& T, unsigned ep, int wid, int lane) {
  constexpr int MODE = 1; (void)MODE;
  const int i16 = lane & 15, kg = lane >> 4, p = wid & 1, ho = (wid >> 1) & 1;
  const __amdgpu_buffer_rsrc_t rh = __builtin_amdgcn_make_buffer_rsrc((void*)(ws + OFF_HALO), (short)0, 256 * HALO_STRIDE, 0x00020000);
  float* red = (float*)(lds + M_RED);
  {
    u32x4 dw2[9];
    u_load(dw2, w2, 1, T, uidx, wid - 4, lane);
    lds_bar();
    bar();
    wait_vm<0>();
    u_landed(dw2);
    u_emit(dw2, ws, 1, T, uidx, wid - 4, lane);
  }
  bar();
  float tot = 0.f, m2 = 0.f;
  if (wid == 4) {
    const float s0 = red[0], s1 = red[1], s2 = red[2], s3 = red[3];
    tot = (s0 + s1) + (s2 + s3);
    const float bm = tot * (1.f / 16384.f);
    const float d0 = s0 * (1.f / 4096.f) - bm, d1 = s1 * (1.f / 4096.f) - bm, d2 = s2 * (1.f / 4096.f) - bm, d3 = s3 * (1.f / 4096.f) - bm;
    m2 = (red[8] + red[9]) + (red[10] + red[11]) + 4096.f * ((d0 * d0 + d1 * d1) + (d2 * d2 + d3 * d3));
    group_stats(ws, 1, T, tot, m2, ep, lane);
    STAMP(14); STAMPR(16);
  }
  wait_vm<0>();
  set_tag(ws, T.bid, W_U2 + (wid - 4), ep, lane);
  if (wid == 4) {
    group_stats_wait(ws, 1, T, tot, m2, ep, red, lane);
    STAMP(15); STAMPR(17);
  }
  const int tyn = p == 0 ? T.ty - 1 : T.ty + 1;
  const bool have_v = tyn >= 0 && tyn < 8;
  const int bid_v = tile_bid(T.b, have_v ? tyn : T.ty, T.tx, T.nt);
  f32x4 ve[2], vo[2];
  const int hbv = bid_v * HALO_STRIDE + (p == 0 ? 8192 : 0) + ho * 4096 + lane * 16;
  if (wid != 4) {
    wait_tags<1>(ws, bid_v, W_HALO, have_v, ep);
#pragma unroll
    for (int f = 0; f < 2; ++f) {
      ve[f] = __builtin_bit_cast(f32x4, __builtin_amdgcn_raw_buffer_load_b128(rh, hbv + f * 2048, 0, 16));
      vo[f] = __builtin_bit_cast(f32x4, __builtin_amdgcn_raw_buffer_load_b128(rh, hbv + f * 2048 + 1024, 0, 16));
    }
  }
  if (wid == 5) {
    const int nb = T.bid ^ 32;
    wait_tags<1>(ws, nb, W_HALO, true, ep);
    const u32x4 c0 = __builtin_amdgcn_raw_buffer_load_b128(rh, nb * HALO_STRIDE + 16384 + lane * 16, 0, 16);
    const u32x4 c1 = __builtin_amdgcn_raw_buffer_load_b128(rh, nb * HALO_STRIDE + 16384 + 1024 + lane * 16, 0, 16);
    *(u32x4*)(lds + M_RAW + 256 + lane * 16) = c0;
    *(u32x4*)(lds + M_RAW + 256 + 1024 + lane * 16) = c1;
  } else if (wid >= 6) {
    const int tyd = wid == 6 ? T.ty - 1 : T.ty + 1;
    float cv = 0.f;
    if (tyd >= 0 && tyd < 8) {
      const int nb = tile_bid(T.b, tyd, T.tx ^ 1, T.nt);
      wait_tags<1>(ws, nb, W_HALO, true, ep);
      cv = __uint_as_float(__builtin_amdgcn_raw_buffer_load_b32(rh, nb * HALO_STRIDE + 16384 + (wid == 6 ? 7 : 0) * 256 + lane * 4, 0, 16));
    }
    ((float*)(lds + M_RAW))[(wid == 6 ? 0 : 9) * 64 + lane] = cv;
  }
  const int chl = lane & 31, half = lane >> 5;
  const float gam_c = ((const float*)(lds + M_GAM))[32 * ho + chl], bet_c = ((const float*)(lds + M_BET))[32 * ho + chl];
  f32x4 he[2], hodd[2];
  const char* hd = lds + X_HAND + (wid - 4) * 4096 + lane * 16;
#pragma unroll
  for (int f = 0; f < 2; ++f) { he[f] = *(const f32x4*)(hd + (f * 2) * 1024); hodd[f] = *(const f32x4*)(hd + (f * 2 + 1) * 1024); }
  lds_bar();
  if (wid == 4) {
    wait_tags<1>(ws, bid_v, W_HALO, have_v, ep);
#pragma unroll
    for (int f = 0; f < 2; ++f) {
      ve[f] = __builtin_bit_cast(f32x4, __builtin_amdgcn_raw_buffer_load_b128(rh, hbv + f * 2048, 0, 16));
      vo[f] = __builtin_bit_cast(f32x4, __builtin_amdgcn_raw_buffer_load_b128(rh, hbv + f * 2048 + 1024, 0, 16));
    }
  }
  Affine af;
  load_affine(af, lds, ho, kg);
  float* col = (float*)(lds + M_COL + wid * 512);
  {
    const float* raw = (const float*)(lds + M_RAW);
    const float sa = red[17] * gam_c, sb = bet_c - red[16] * sa;
    col[lane] = silu_f(raw[(half == 0 ? 4 + p : 9 * p) * 64 + 32 * ho + chl] * sa + sb);
  }
  const __amdgpu_buffer_rsrc_t rv = __builtin_amdgcn_make_buffer_rsrc((void*)(ws + OFF_V2), (short)0, (int)V_BYTES, 0x00020000);
  f32x4 hl[2], hr[2];
  u32x4 o[4];
  if (wid == 5) {
    wait_tags<1>(ws, uprod, W_U2, lane < 32 && lane != uidx, ep);
    lds_flag_set(lds, 3, ep);
    ld_u(L, 0); ld_u(L, 1); ld_u(L, 2);
  }
  halo_ops(lds, col, T.tx, i16, kg, hl, hr);
  row_transform(he, hodd, hl, hr, af, o);
  put_row(lds, rv, T, ho, 4 + p, true, i16, kg, o);
  asm volatile("" : "+v"(ve[0]), "+v"(ve[1]), "+v"(vo[0]), "+v"(vo[1]));
  if (wid != 5) {
    lds_flag_wait(lds, 3, ep);
    ld_u(L, 0); ld_u(L, 1); ld_u(L, 2);
  }
  halo_ops(lds, col + 32, T.tx, i16, kg, hl, hr);
  row_transform(ve, vo, hl, hr, af, o);
  if (!have_v) { o[0] = o[1] = o[2] = o[3] = u32x4{0u, 0u, 0u, 0u}; }
  put_row(lds, rv, T, ho, 9 * p, false, i16, kg, o);
  if (wid == 5) wait_vm<4>(); else wait_vm<0>();
}

constexpr int xpiece(int k) { return k < 24 ? LDS_U + 2 * USLOT + k * 1024 : LDS_U + (k - 24) * 1024; }

template <int P> DI void epi2_mfma(const Comp& s, char* lds, char* ws, const Tile& T, u16* __restrict__ out, int wid, int lane) {
  constexpr int MODE = 2; (void)MODE; (void)ws;
  const int i16 = lane & 15, kg = lane >> 4, ho = wid >> 1;
  bar();
  f32x4 ye[4][2], yo[4][2];
  exchange<P>(s, lds, wid, lane, ye, yo);
  if (wid == 0) { STAMP(12); }
  const size_t off0 = ((size_t)(T.b * NCH + T.nt * 64 + 32 * ho + i16)) * HW + (T.y0 + 4 * P) * 64 + 32 * T.tx + 8 * kg;
#pragma unroll
  for (int f = 0; f < 2; ++f)
#pragma unroll
    for (int r = 0; r < 4; ++r) {
      const int k = (ho * 2 + f) * 8 + 4 * P + r;
      const u32x4 xv = *(const u32x4*)(lds + (k < 24 ? LDS_U + 2 * USLOT + k * 1024 : LDS_U + (k - 24) * 1024) + lane * 16);
      u32x4 o;
#pragma unroll
      for (int j = 0; j < 4; ++j) o[j] = pack_bf16(ye[r][f][j] + bf_lo(xv[j]), yo[r][f][j] + bf_hi(xv[j]));
      *(u32x4*)(out + off0 + (size_t)f * 16 * HW + r * 64) = o;
    }
}

template <int MODE> DI void loader_loop(const Loader& L, int uidx, int uprod, u32x4 (&ep8)[8], char* lds, char* ws, const Tile& T, unsigned ep, int w, int lane) {
  const int pty = T.ty - 1 + (lane >> 3);
  const bool need = lane < 24 && pty >= 0 && pty < 8 && (lane & 7) != T.nt;
  const int pbid = need ? tile_bid(T.b, pty, T.tx, lane & 7) : T.bid;
  constexpr int WV = MODE == 1 ? W_V1 : W_V2;
  lds_bar();
  if (w == 0) { STAMPR(20); }
  u32x4 tg0 = {0u, 0u, 0u, 0u}, tg1 = {0u, 0u, 0u, 0u};
  if (w == 0) {
    const char* tp = ws + OFF_LINES + pbid * 128 + WV * 4;
    asm volatile("global_load_dwordx4 %0, %2, off sc1\n\tglobal_load_dwordx4 %1, %2, off offset:16 sc1" : "=&v"(tg0), "=&v"(tg1) : "v"(tp) : "memory");
  }
  bar();
  ld_u(L, 3);
  if constexpr (MODE == 2) {
    if (w == 0) wait_vm<8>(); else wait_vm<6>();
    set_tag(ws, T.bid, WV + 4 + w, ep, lane);
  } else {
    if (w == 1) { wait_tags<1>(ws, uprod, W_U1, lane < 32 && lane != uidx, ep); lds_flag_set(lds, 12, ep); }
    if (w == 3) {
      lds_flag_wait(lds, 12, ep);
      const __amdgpu_buffer_rsrc_t re = __builtin_amdgcn_make_buffer_rsrc((void*)(ws + OFF_EMBP), (short)0, 8 * 32 * 512, 0x00020000);
#pragma unroll
      for (int i = 0; i < 8; ++i) ep8[i] = __builtin_amdgcn_raw_buffer_load_b128(re, (T.nt * 32 + (lane >> 4) * 8 + i) * 512 + T.b * 256 + (lane & 15) * 16, 0, 16);
    }
  }
  bar();
  ld_u(L, 4);
  bool early = false;
  if (w == 0) {
    wait_vm<MODE == 2 ? 13 : 12>();
    asm volatile("" : "+v"(tg0), "+v"(tg1) :: "memory");
    early = __all(!need || (tg0[0] == ep && tg0[1] == ep && tg0[2] == ep && tg0[3] == ep && tg1[0] == ep && tg1[1] == ep && tg1[2] == ep && tg1[3] == ep));
    if (early) lds_flag_set(lds, MODE, ep);
  }
  wait_vm<MODE == 2 ? 7 : 6>();
  bar();
  if (w == 0) {
    STAMPR(22);
    if (!early) { wait_tags<2>(ws, pbid, WV, need, ep); lds_flag_set(lds, MODE, ep); }
    STAMPR(23);
  } else {
    lds_flag_wait(lds, MODE, ep);
  }
  ld_v(L, 4); ld_v(L, 5); ld_u(L, 5);
  wait_vm<11>();
  bar();
#pragma unroll 1
  for (int t = 4; t < NUNIT - 2; ++t) {
    ld_v(L, t + 2); ld_u(L, t + 2);
    wait_vm<11>();
    bar();
  }
}

template <int MODE> DI void mfma_loop(Comp& s, char* ws, int bid, unsigned ep, int wid, int lane) {
  if constexpr (MODE == 1) {
    wait_vm<0>();
    set_tag(ws, bid, W_V1 + wid, ep, lane);
  }
  lds_bar();
  lds_rd<0>(s.B[0][0], s.bbase); lds_rd<1024>(s.B[0][1], s.bbase); lds_rd<0>(s.A[0], s.abase);
  lds_rd<4096>(s.B[0][2], s.bbase); lds_rd<5120>(s.B[0][3], s.bbase); lds_rd<8192>(s.B[0][4], s.bbase); lds_rd<9216>(s.B[0][5], s.bbase);
  lds_rd<1024>(s.A[1], s.abase); lds_rd<2048>(s.A[2], s.abase); lds_rd<3072>(s.A[3], s.abase);
  wait_lgkm<3>();
  s.anxt = s.abase;
  unsigned voff = 0, uoff = 0;
#pragma unroll 1
  for (int c = 0; c < 16; ++c) {
    unit<MODE, 0>(s, voff, uoff);
    if (MODE == 2 && c == 0) {
      wait_vm<0>();
      set_tag(ws, bid, (MODE == 1 ? W_V1 : W_V2) + wid, ep, lane);
    }
    unit<MODE, 1>(s, voff, uoff);
  }
  asm volatile("s_waitcnt lgkmcnt(0)\n\ts_nop 15" ::: "memory");
}
DI void clear_acc(Comp& s) {
#pragma unroll
  for (int e = 0; e < 2; ++e)
#pragma unroll
    for (int r = 0; r < 8; ++r)
#pragma unroll
      for (int o = 0; o < 2; ++o) { s.acc[e][r][o] = f32x4{0.f, 0.f, 0.f, 0.f}; asm volatile("" : "+v"(s.acc[e][r][o])); }
}

__global__ __launch_bounds__(512, 2) void k_main(const u16* __restrict__ x, const u16* __restrict__ temb, const u16* __restrict__ g1, const u16* __restrict__ be1,
                                                 const u16* __restrict__ w1, const u16* __restrict__ wemb, const u16* __restrict__ g2,
                                                 const u16* __restrict__ be2, const u16* __restrict__ w2, u16* __restrict__ out, char* __restrict__ ws) {
  __shared__ __attribute__((aligned(16))) char lds[LDS_TOTAL];
  const int tid = threadIdx.x, lane = tid & 63;
  const int wid = __builtin_amdgcn_readfirstlane(tid >> 6);
  const int bid = blockIdx.x, xcd = bid & 7, slot = bid >> 3;
  Tile T;
  T.bid = bid; T.b = xcd >> 2; T.nt = (xcd & 1) * 4 + (slot & 3); T.ty = ((xcd >> 1) & 1) * 4 + (slot >> 3); T.tx = (slot >> 2) & 1; T.y0 = T.ty * 8;
  const unsigned ldsb = (unsigned)(size_t)lds;
  const int p = wid & 1, ho = (wid >> 1) & 1;
  const unsigned ep = __builtin_amdgcn_readfirstlane(__hip_atomic_load((gu32*)(ws + OFF_EPOCH), RLX_AGENT)) + 1u;
  float* red = (float*)(lds + M_RED);

  if (wid >= 4) {
    const int w = wid - 4;
    const int uidx = (T.b * 8 + T.ty) * 2 + T.tx;
    const int rho[2] = {4 + p, 9 * p};
    const char* stage = lds + LDS_U + w * 9216;
    u_stage(ldsb + LDS_U + w * 9216, w1, 0, T, uidx, w, lane);
    XRows<2> X;
    rows_load<2>(X, rho, (lane >> 5) ? 9 * p : 4 + p, 0, x, g1, be1, T, ho, lane);
    u32x4 we[3];
    unsigned short te[2][3];
    if (w == 3) {
#pragma unroll
      for (int i = 0; i < 3; ++i) {
        const int k = 24 * uidx + 3 * (lane >> 3) + i;
        we[i] = *(const u32x4*)(wemb + (size_t)k * 512 + T.nt * 64 + 8 * (lane & 7));
        te[0][i] = temb[k]; te[1][i] = temb[NEMB + k];
      }
    }
    rows_stats<2, 1>(X, red, wid, lane);
    asm volatile("s_waitcnt lgkmcnt(0)" ::: "memory");
    lds_flag_set(lds, 4 + wid, ep);
    { constexpr int MODE = 1; (void)MODE; if (w == 1) { STAMPR(48); } }
    {
      u32x4 dw1[9];
      u_fetch(dw1, stage, lane);
      u_emit(dw1, ws, 0, T, uidx, w, lane);
    }
    { constexpr int MODE = 1; (void)MODE; if (w == 1) { STAMPR(50); } }
    if (w == 3) {
      float a[2][8];
#pragma unroll
      for (int j = 0; j < 8; ++j) a[0][j] = a[1][j] = 0.f;
#pragma unroll
      for (int i = 0; i < 3; ++i) {
        const float t0 = silu_f(__uint_as_float((unsigned)te[0][i] << 16)), t1 = silu_f(__uint_as_float((unsigned)te[1][i] << 16));
#pragma unroll
        for (int j = 0; j < 8; ++j) {
          const float wv = (j & 1) ? bf_hi(we[i][j >> 1]) : bf_lo(we[i][j >> 1]);
          a[0][j] += t0 * wv; a[1][j] += t1 * wv;
        }
      }
      float* sc = (float*)(lds + M_COL);
#pragma unroll
      for (int j = 0; j < 8; ++j) { sc[(lane >> 3) * 128 + (lane & 7) * 8 + j] = a[0][j]; sc[(lane >> 3) * 128 + 64 + (lane & 7) * 8 + j] = a[1][j]; }
      float e0 = 0.f, e1 = 0.f;
#pragma unroll
      for (int g = 0; g < 8; ++g) { e0 += sc[g * 128 + lane]; e1 += sc[g * 128 + 64 + lane]; }
      const __amdgpu_buffer_rsrc_t re = __builtin_amdgcn_make_buffer_rsrc((void*)(ws + OFF_EMBP), (short)0, 8 * 32 * 512, 0x00020000);
      __builtin_amdgcn_raw_buffer_store_b32(__float_as_uint(e0), re, (T.nt * 32 + uidx) * 512 + lane * 4, 0, 16);
      __builtin_amdgcn_raw_buffer_store_b32(__float_as_uint(e1), re, (T.nt * 32 + uidx) * 512 + 256 + lane * 4, 0, 16);
    }
    wait_vm<0>();
    set_tag(ws, T.bid, W_U1 + w, ep, lane);
    {
      const __amdgpu_buffer_rsrc_t rw = __builtin_amdgcn_make_buffer_rsrc((void*)w2, (short)0, 512 * 512 * 9 * 2, 0x00020000);
#pragma unroll
      for (int h = 0; h < 2; ++h) {
        const int t = min(64 * h + lane, 79), row = t / 5;
        const unsigned vo = (unsigned)(((T.nt * 64 + u_row(1, w, row)) * 512 + 32 * (uidx >> 1)) * 18 + (t - row * 5) * 128);
        asm volatile("s_mov_b32 m0, %0\n\ts_nop 0\n\tbuffer_load_dword %1, %2, 0 offen lds" :: "s"(ldsb + M_DUMP), "v"(vo), "s"(rw) : "memory");
      }
    }
    { constexpr int MODE = 1; (void)MODE; if (w == 1) { STAMPR(52); } }
    lds_bar();
    Loader L;
    L.nt = T.nt;
    L.rv = __builtin_amdgcn_make_buffer_rsrc((void*)(ws + OFF_V1), (short)0, (int)V_BYTES, 0x00020000);
    L.ru = __builtin_amdgcn_make_buffer_rsrc((void*)(ws + OFF_U1), (short)0, (int)U_BYTES, 0x00020000);
    L.vmid = lane * 16;
    L.vfirst = (T.y0 == 0 && !(w & 1)) ? 0x80000000u : L.vmid;
    L.vlast = (T.y0 == 56 && (w & 1)) ? 0x80000000u : L.vmid;
    L.sv = (unsigned)T.b * 16u * 524288u + (unsigned)(w >> 1) * 131072u + (unsigned)((T.y0 + 5 * (w & 1)) * 2048 + T.tx * 1024) - 2048u;
    L.su = (unsigned)T.nt * 786432u + (unsigned)w * 6144u;
    L.mv = ldsb + (w >> 1) * 10240 + (w & 1) * 5120;
    L.mu = ldsb + LDS_U + w * 6144;
    const int uprod = tile_bid((lane >> 4) & 1, (lane >> 1) & 7, lane & 1, T.nt);
    rows_column<2>(X, lds, wid, lane);
    if (w == 1) {
      const int sl = 4 * T.nt + (lane & 3);
      wait_tags<1>(ws, tile_bid(sl >> 4, (sl >> 1) & 7, sl & 1, T.nt), W_U1, lane < 4 && sl != uidx, ep);
      lds_flag_set(lds, 0, ep);
    } else {
      rows_emit<2>(X, rho, 0, 1, lds, ws, T, wid, ho, lane);
      wait_vm<0>();
      set_tag(ws, T.bid, W_V1 + 4 + w, ep, lane);
      lds_flag_wait(lds, 0, ep);
    }
    { constexpr int MODE = 1; (void)MODE; if (w == 1) { STAMPR(53); } }
    ld_u(L, 0); ld_u(L, 1); ld_u(L, 2);
    rows_emit<2>(X, rho, 1, 2, lds, ws, T, wid, ho, lane);
    if (w == 1) rows_emit<2>(X, rho, 0, 1, lds, ws, T, wid, ho, lane);
    wait_vm<0>();
    if (w == 1) set_tag(ws, T.bid, W_V1 + 4 + w, ep, lane);
    u32x4 ep8[8];
    loader_loop<1>(L, uidx, uprod, ep8, lds, ws, T, ep, w, lane);
    if (w == 3) {
      f32x4 e = {0.f, 0.f, 0.f, 0.f};
#pragma unroll
      for (int i = 0; i < 8; ++i) e += __builtin_bit_cast(f32x4, ep8[i]);
#pragma unroll
      for (int j = 0; j < 4; ++j) { e[j] += __shfl_xor(e[j], 16); e[j] += __shfl_xor(e[j], 32); }
      if (lane < 16) *(f32x4*)(lds + M_EMB + lane * 16) = e;
    }
    wait_vm<0>();
    bar();
    bar();
    L.rv = __builtin_amdgcn_make_buffer_rsrc((void*)(ws + OFF_V2), (short)0, (int)V_BYTES, 0x00020000);
    L.ru = __builtin_amdgcn_make_buffer_rsrc((void*)(ws + OFF_U2), (short)0, (int)U_BYTES, 0x00020000);
    epi1_loader(L, w2, uprod, uidx, lds, ws, T, ep, wid, lane);
    loader_loop<2>(L, uidx, uprod, ep8, lds, ws, T, ep, w, lane);
    {
      const __amdgpu_buffer_rsrc_t rx = __builtin_amdgcn_make_buffer_rsrc((void*)x, (short)0, 2 * NCH * HW * 2, 0x00020000);
      const unsigned vx = (lane & 15) * (HW * 2) + (lane >> 4) * 16;
      const unsigned sx = (unsigned)((T.b * NCH + T.nt * 64) * HW + T.y0 * 64 + 32 * T.tx) * 2u;
#pragma unroll
      for (int q = 0; q < 6; ++q) { const int k = w * 6 + q; dma1(ldsb + xpiece(0) + k * 1024, vx, rx, sx + (unsigned)(k >> 3) * 16u * HW * 2u + (unsigned)(k & 7) * 128u); }
      wait_vm<6>();
      bar();
#pragma unroll
      for (int q = 0; q < 2; ++q) { const int k = 24 + w * 2 + q; dma1(ldsb + xpiece(24) + (k - 24) * 1024, vx, rx, sx + (unsigned)(k >> 3) * 16u * HW * 2u + (unsigned)(k & 7) * 128u); }
      bar();
      wait_vm<0>();
      bar();
    }
    return;
  }

  const int i16 = lane & 15, kg = lane >> 4;
  {
    constexpr int MODE = 1; (void)MODE;
    if (wid == 0) { STAMP(0); STAMPR(4); }
    const int rho[3] = {5 * p + 1, 5 * p + 2, 5 * p + 3};
    XRows<3> X;
    rows_load<3>(X, rho, 5 * p + 1 + (lane >> 5), 5 * p + 3, x, g1, be1, T, ho, lane);
    unsigned short g2c = 0, b2c = 0;
    if (wid == 1) { g2c = g2[T.nt * 64 + lane]; b2c = be2[T.nt * 64 + lane]; }
    rows_stats<3, 3>(X, red, wid, lane);
    if (wid == 1) {
      if (lane < 8) ((float*)(lds + M_ZERO))[lane] = 0.f;
      ((float*)(lds + M_GAM))[lane] = __uint_as_float((unsigned)g2c << 16);
      ((float*)(lds + M_BET))[lane] = __uint_as_float((unsigned)b2c << 16);
    }
    asm volatile("s_waitcnt lgkmcnt(0)" ::: "memory");
    lds_flag_set(lds, 4 + wid, ep);
    if (wid == 0) {
      STAMP(24);
#pragma unroll
      for (int k = 1; k < 8; ++k) lds_flag_wait(lds, 4 + k, ep);
      float tot = 0.f;
#pragma unroll
      for (int k = 0; k < 8; ++k) tot += red[k];
      const float bm = tot * (1.f / 16384.f);
      float m2 = 0.f;
#pragma unroll
      for (int k = 0; k < 8; ++k) {
        const float n = k < 4 ? 3072.f : 1024.f, d = red[k] * (k < 4 ? 1.f / 3072.f : 1.f / 1024.f) - bm;
        m2 += red[8 + k] + n * d * d;
      }
      group_stats(ws, 0, T, tot, m2, ep, lane);
      group_stats_wait(ws, 0, T, tot, m2, ep, red, lane);
      STAMPR(54);
    }
    lds_bar();
    if (wid == 0) { STAMP(25); }
    rows_column<3>(X, lds, wid, lane);
    rows_emit<3>(X, rho, 0, 3, lds, ws, T, wid, ho, lane);
  }
  Comp s;
  s.abase = ldsb + p * 10240 + i16 * 64 + vpos(kg, i16) * 16;
  s.bbase = ldsb + LDS_U + p * 12288 + ho * 2048 + lane * 16;
  clear_acc(s);
  {
    constexpr int MODE = 1; (void)MODE;
    if (wid == 0) { STAMP(1); }
    mfma_loop<1>(s, ws, bid, ep, wid, lane);
    if (wid == 0) { STAMP(2); STAMPR(5); }
    bar();
    if (p == 0) epi1_mfma<0>(s, lds, ws, T, ep, wid, lane); else epi1_mfma<1>(s, lds, ws, T, ep, wid, lane);
  }
  {
    constexpr int MODE = 2; (void)MODE;
    if (wid == 0) { STAMP(0); STAMPR(4); }
    clear_acc(s);
    if (wid == 0) { STAMP(1); }
    mfma_loop<2>(s, ws, bid, ep, wid, lane);
    if (wid == 0) { STAMP(2); STAMPR(5); }
    if (p == 0) epi2_mfma<0>(s, lds, ws, T, out, wid, lane); else epi2_mfma<1>(s, lds, ws, T, out, wid, lane);
  }
  if (wid == 0 && lane < 16) ((unsigned*)(lds + M_RED + 96))[lane] = 0u;
  if (bid == 0 && tid == 0) __hip_atomic_store((gu32*)(ws + OFF_EPOCH), ep, RLX_AGENT);
}

extern "C" void kernel_launch(void* const* d_in, const int* in_sizes, int n_in, void* d_out, int out_size, void* d_ws, size_t ws_size,
                              hipStream_t stream) {
  k_main<<<256, 512, 0, stream>>>((const u16*)d_in[0], (const u16*)d_in[1], (const u16*)d_in[2], (const u16*)d_in[3], (const u16*)d_in[4],
                                  (const u16*)d_in[5], (const u16*)d_in[6], (const u16*)d_in[7], (const u16*)d_in[8], (u16*)d_out, (char*)d_ws);
}
```

```cpp
#include <hip/hip_runtime.h>
#include <hip/hip_bf16.h>
#include <stdint.h>
#include <utility>

#define DI __device__ __forceinline__
typedef __bf16 bf16x8 __attribute__((ext_vector_type(8)));
typedef __bf16 bf16x2 __attribute__((ext_vector_type(2)));
typedef float f32x4 __attribute__((ext_vector_type(4)));
typedef float f32x2 __attribute__((ext_vector_type(2)));
typedef unsigned u32x4 __attribute__((ext_vector_type(4)));
typedef unsigned u32x2 __attribute__((ext_vector_type(2)));
typedef unsigned short u16;
#define GAS __attribute__((address_space(1)))
typedef GAS unsigned long long gu64;
typedef GAS unsigned gu32;
#define RLX_AGENT __ATOMIC_RELAXED, __HIP_MEMORY_SCOPE_AGENT

constexpr int NCH = 512, HW = 4096, NEMB = 768;
constexpr float GN_EPS = 1e-5f;
constexpr unsigned SPIN_CAP = 1u << 20;
#define V_STORE_AUX 16
#define OUT_AUX 16

constexpr size_t U_BYTES = (size_t)8 * 16 * 49152;
constexpr size_t V_BYTES = (size_t)2 * 16 * 4 * 131072;
constexpr size_t OFF_U1 = 0, OFF_U2 = U_BYTES;
constexpr size_t OFF_V1 = 2 * U_BYTES, OFF_V2 = OFF_V1 + V_BYTES;
constexpr size_t OFF_EPOCH = OFF_V2 + V_BYTES;
constexpr size_t OFF_GRAN = OFF_EPOCH + 256;
constexpr size_t OFF_LINES = OFF_GRAN + 8192;
constexpr int W_HALO = 0, W_V1 = 4, W_V2 = 12, W_U1 = 20, W_U2 = 24;
constexpr size_t OFF_EMBP = OFF_LINES + 256 * 128;
constexpr size_t OFF_HALO = OFF_EMBP + 8 * 32 * 512;
constexpr int HALO_STRIDE = 18432;
constexpr size_t OFF_DIAG = OFF_HALO + 256 * HALO_STRIDE;
#define STAMP(i)
#define STAMPR(i)

DI float bf_lo(unsigned v) { return __uint_as_float(v << 16); }
DI float bf_hi(unsigned v) { return __uint_as_float(v & 0xffff0000u); }
DI unsigned pack_bf16(float lo, float hi) {
  f32x2 v = {lo, hi};
  bf16x2 b = __builtin_convertvector(v, bf16x2);
  return __builtin_bit_cast(unsigned, b);
}
DI float silu_f(float y) { return y * __builtin_amdgcn_rcpf(1.f + __builtin_amdgcn_exp2f(y * -1.44269504088896341f)); }
DI f32x2 pk_sub(f32x2 a, f32x2 b) { return __builtin_elementwise_fma(b, f32x2{-1.f, -1.f}, a); }
DI f32x2 lo2(const f32x4& v) { return __builtin_shufflevector(v, v, 0, 1); }
DI f32x2 hi2(const f32x4& v) { return __builtin_shufflevector(v, v, 2, 3); }
DI f32x4 cat2(f32x2 a, f32x2 b) { return __builtin_shufflevector(a, b, 0, 1, 2, 3); }
DI f32x4 sub4(const f32x4& a, const f32x4& b) { return cat2(pk_sub(lo2(a), lo2(b)), pk_sub(hi2(a), hi2(b))); }
DI f32x2 silu_aff2(f32x2 h, f32x2 sa, f32x2 sb, f32x2 sc, f32x2 sd) {
  const f32x2 y = __builtin_elementwise_fma(h, sa, sb), t = __builtin_elementwise_fma(h, sc, sd);
  const f32x2 d = f32x2{__builtin_amdgcn_exp2f(t[0]), __builtin_amdgcn_exp2f(t[1])} + 1.f;
  return y * f32x2{__builtin_amdgcn_rcpf(d[0]), __builtin_amdgcn_rcpf(d[1])};
}
template <int CTRL, int RM = 0xf> DI float dpp0(float v) {
  return __builtin_bit_cast(float, __builtin_amdgcn_update_dpp(0, __builtin_bit_cast(int, v), CTRL, RM, 0xf, true));
}
DI float dpp_prev(float v) { return dpp0<0x111>(v); }
DI float dpp_next(float v) { return dpp0<0x101>(v); }
DI float wave_sum(float v) {
  v += dpp0<0x111>(v); v += dpp0<0x112>(v); v += dpp0<0x114>(v); v += dpp0<0x118>(v);
  v += dpp0<0x142, 0xa>(v);
  v += dpp0<0x143, 0xc>(v);
  return __builtin_bit_cast(float, __builtin_amdgcn_readlane(__builtin_bit_cast(int, v), 63));
}
DI int vpos(int o, int xt) { return (((o & 1) << 1) | (o >> 1)) ^ ((xt >> 2) & 1); }
DI void bar() { asm volatile("s_barrier" ::: "memory"); }
DI void lds_bar() { asm volatile("s_waitcnt lgkmcnt(0)\n\ts_barrier" ::: "memory"); }
template <int N> DI void wait_vm() { asm volatile("s_waitcnt vmcnt(%0)" :: "i"(N) : "memory"); }

DI void gn_combine(float s, float q, bool on, float n, float& mean, float& rstd) {
  const float tot = wave_sum(on ? s : 0.f);
  mean = tot * (1.f / (32.f * n));
  const float d = s * (1.f / n) - mean;
  const float m2 = wave_sum(on ? (q + n * d * d) : 0.f);
  rstd = __builtin_amdgcn_rsqf(m2 * (1.f / (32.f * n)) + GN_EPS);
}

constexpr int VSLOT = 20480, NVSLOT = 4;
constexpr int USLOT = 24576, NUSLOT = 3;
constexpr int LDS_U = NVSLOT * VSLOT, LDS_MISC = LDS_U + NUSLOT * USLOT, LDS_TOTAL = 163840;
constexpr int NUNIT = 32;
constexpr int M_EMB = LDS_MISC, M_GAM = LDS_MISC + 256, M_BET = LDS_MISC + 512, M_RED = LDS_MISC + 768, M_ZERO = LDS_MISC + 960, M_RAW = LDS_MISC + 1024, M_COL = LDS_MISC + 3584, M_DUMP = LDS_MISC + 7680;
constexpr int X_HAND = 65536;

struct Tile { int bid, b, nt, ty, tx, y0; };
DI int tile_bid(int b, int ty, int tx, int nt) { return (((ty & 3) * 8 + tx * 4 + (nt & 3)) << 3) | (b * 4 + (ty >> 2) * 2 + (nt >> 2)); }
DI int chunk_of(int j, int nt) { return j < 2 ? 2 * nt + j : (j - 2 < 2 * nt ? j - 2 : j); }

struct Loader {
  __amdgpu_buffer_rsrc_t rv, ru;
  unsigned vfirst, vmid, vlast;
  unsigned sv, su;
  unsigned mv, mu;
  int nt;
};
DI void dma1(unsigned m0, unsigned voff, __amdgpu_buffer_rsrc_t r, unsigned soff) {
  asm volatile("s_mov_b32 m0, %0\n\ts_nop 0\n\tbuffer_load_dwordx4 %1, %2, %3 offen lds" :: "s"(m0), "v"(voff), "s"(r), "s"(soff) : "memory");
}
DI void dma1_dev(unsigned m0, unsigned voff, __amdgpu_buffer_rsrc_t r, unsigned soff) {
  asm volatile("s_mov_b32 m0, %0\n\ts_nop 0\n\tbuffer_load_dwordx4 %1, %2, %3 offen sc1 lds" :: "s"(m0), "v"(voff), "s"(r), "s"(soff) : "memory");
}
DI void ld_u(const Loader& L, int k) {
  const unsigned mu = L.mu + (unsigned)(k % NUSLOT) * USLOT, su = L.su + (unsigned)(chunk_of(k >> 1, L.nt) * 2 + (k & 1)) * USLOT;
  asm volatile("s_mov_b32 m0, %0\n\ts_nop 0\n\t"
               "buffer_load_dwordx4 %1, %2, %3 offen sc1 lds\n\t"
               "buffer_load_dwordx4 %1, %2, %3 offen offset:1024 sc1 lds\n\t"
               "buffer_load_dwordx4 %1, %2, %3 offen offset:2048 sc1 lds\n\t"
               "buffer_load_dwordx4 %1, %2, %3 offen offset:3072 sc1 lds"
               :: "s"(mu), "v"(L.vmid), "s"(L.ru), "s"(su) : "memory");
  asm volatile("s_mov_b32 m0, %0\n\ts_nop 0\n\t"
               "buffer_load_dwordx4 %1, %2, %3 offen sc1 lds\n\t"
               "buffer_load_dwordx4 %1, %2, %3 offen offset:1024 sc1 lds"
               :: "s"(mu + 4096), "v"(L.vmid), "s"(L.ru), "s"(su + 4096) : "memory");
}
DI void ld_v(const Loader& L, int k) {
  const unsigned mv = L.mv + (unsigned)(k & 3) * VSLOT, sv = L.sv + (unsigned)(chunk_of(k >> 1, L.nt) * 2 + (k & 1)) * 262144u;
  dma1_dev(mv, L.vfirst, L.rv, sv);
  dma1_dev(mv + 1024, L.vmid, L.rv, sv + 2048);
  dma1_dev(mv + 2048, L.vmid, L.rv, sv + 4096);
  dma1_dev(mv + 3072, L.vmid, L.rv, sv + 6144);
  dma1_dev(mv + 4096, L.vlast, L.rv, sv + 8192);
}

struct Comp {
  f32x4 acc[2][8][2];
  bf16x8 A[10];
  bf16x8 B[2][6];
  unsigned abase, bbase;
  unsigned acur, anxt, bnxt;
};
struct MD { int r, dy, o; };
constexpr MD mdecode(int m) {
  int n = 0;
  for (int r = 0; r < 10; ++r)
    for (int dy = 0; dy < 3; ++dy) {
      if (r - dy < 0 || r - dy > 7) continue;
      for (int o = 0; o < 2; ++o) { if (n == m) return MD{r, dy, o}; ++n; }
    }
  return MD{-1, -1, -1};
}
template <int OFF> DI void lds_rd(bf16x8& dst, unsigned addr) { asm volatile("ds_read_b128 %0, %1 offset:%2" : "=v"(dst) : "v"(addr), "i"(OFF)); }
template <int N> DI void wait_lgkm() { asm volatile("s_waitcnt lgkmcnt(%0)" :: "i"(N) : "memory"); }

constexpr int first_wait(int m) {
  switch (m) { case 2: return 3; case 6: return 3; case 12: return 4; case 18: return 5; case 24: return 6; case 30: return 7;
               case 36: return 8; case 42: return 9; case 46: return 10; default: return -1; }
}
template <int MODE, int E, bool FIRST, int M> DI void mstep(Comp& s) {
  constexpr MD d = mdecode(M);
  if constexpr (first_wait(M) >= 0) wait_lgkm<first_wait(M)>();
  if constexpr (FIRST && d.dy == 0) {
    if constexpr (MODE == 1) asm volatile("v_mfma_f32_16x16x32_bf16 %0, %1, %2, 0" : "=&v"(s.acc[E][d.r][d.o]) : "v"(s.B[E][d.o]), "v"(s.A[d.r]));
    else                     asm volatile("v_mfma_f32_16x16x32_bf16 %0, %1, %2, 0" : "=&v"(s.acc[E][d.r][d.o]) : "v"(s.A[d.r]), "v"(s.B[E][d.o]));
  } else {
    if constexpr (MODE == 1) asm volatile("v_mfma_f32_16x16x32_bf16 %0, %1, %2, %0" : "+v"(s.acc[E][d.r - d.dy][d.o]) : "v"(s.B[E][d.dy * 2 + d.o]), "v"(s.A[d.r]));
    else                     asm volatile("v_mfma_f32_16x16x32_bf16 %0, %1, %2, %0" : "+v"(s.acc[E][d.r - d.dy][d.o]) : "v"(s.A[d.r]), "v"(s.B[E][d.dy * 2 + d.o]));
  }
  if constexpr (M % 3 == 0) {
    constexpr int j = M / 3;
    if constexpr (j < 6) lds_rd<(4 + j) * 1024>(s.A[4 + j], s.acur);
    else if constexpr (j == 6) lds_rd<0>(s.B[E ^ 1][0], s.bnxt);
    else if constexpr (j == 7) lds_rd<1024>(s.B[E ^ 1][1], s.bnxt);
    else if constexpr (j == 8) lds_rd<0>(s.A[0], s.anxt);
    else if constexpr (j == 9) lds_rd<4096>(s.B[E ^ 1][2], s.bnxt);
    else if constexpr (j == 10) lds_rd<4096 + 1024>(s.B[E ^ 1][3], s.bnxt);
    else if constexpr (j == 11) lds_rd<8192>(s.B[E ^ 1][4], s.bnxt);
    else if constexpr (j == 12) lds_rd<8192 + 1024>(s.B[E ^ 1][5], s.bnxt);
    else lds_rd<(j - 12) * 1024>(s.A[j - 12], s.anxt);
  }
}
template <int MODE, int E, bool FIRST, int... Ms> DI void msteps(Comp& s, std::integer_sequence<int, Ms...>) { (mstep<MODE, E, FIRST, Ms>(s), ...); }

template <int MODE, int E, bool FIRST = false> DI void unit(Comp& s, unsigned& voff, unsigned& uoff) {
  bar();
  s.acur = s.anxt;
  voff = (voff + VSLOT) == NVSLOT * VSLOT ? 0u : voff + VSLOT;
  s.anxt = s.abase + voff;
  uoff = (uoff + USLOT) == NUSLOT * USLOT ? 0u : uoff + USLOT;
  s.bnxt = s.bbase + uoff;
  msteps<MODE, E, FIRST>(s, std::make_integer_sequence<int, 48>{});
  wait_lgkm<3>();
}

struct Affine { f32x2 sa[4], sb[4], sc[4], sd[4]; };
DI void row_transform(const f32x4 (&ye)[2], const f32x4 (&yo)[2], const f32x4 (&hl)[2], const f32x4 (&hr)[2], const Affine& af, u32x4 (&o)[4]) {
#pragma unroll
  for (int q = 0; q < 4; ++q) {
    const f32x2 he = (q & 1) ? hi2(ye[q >> 1]) : lo2(ye[q >> 1]), ho2 = (q & 1) ? hi2(yo[q >> 1]) : lo2(yo[q >> 1]);
    const f32x2 d1 = silu_aff2(he, af.sa[q], af.sb[q], af.sc[q], af.sd[q]);
    const f32x2 d2 = silu_aff2(ho2, af.sa[q], af.sb[q], af.sc[q], af.sd[q]);
    const f32x2 d2l = pk_sub(d2, (q & 1) ? hi2(hl[q >> 1]) : lo2(hl[q >> 1])), d1r = pk_sub(d1, (q & 1) ? hi2(hr[q >> 1]) : lo2(hr[q >> 1]));
    const f32x2 t1 = d1 + d2, t2 = pk_sub(d2, d1);
    o[0][q] = pack_bf16(dpp_prev(d2[0]) - d2l[0], dpp_prev(d2[1]) - d2l[1]);
    o[1][q] = pack_bf16(t1[0], t1[1]);
    o[2][q] = pack_bf16(t2[0], t2[1]);
    o[3][q] = pack_bf16(d1r[0] - dpp_next(d1[0]), d1r[1] - dpp_next(d1[1]));
  }
}
DI void halo_ops(const char* lds, const float* c, int tx, int i16, int kg, f32x4 (&hl)[2], f32x4 (&hr)[2]) {
  const float* z = (const float*)(lds + M_ZERO);
  const float* pl = (tx == 1 && i16 == 0) ? c + 8 * kg : z;
  const float* pr = (tx == 0 && i16 == 15) ? c + 8 * kg : z;
  hl[0] = *(const f32x4*)pl; hl[1] = *(const f32x4*)(pl + 4);
  hr[0] = *(const f32x4*)pr; hr[1] = *(const f32x4*)(pr + 4);
}

template <int P> DI void exchange(const Comp& s, char* lds, int wid, int lane, f32x4 (&ye)[4][2], f32x4 (&yo)[4][2]) {
  char* xb = lds + wid * 16384 + lane * 16;
  const char* rb = lds + (wid ^ 1) * 16384 + lane * 16;
#pragma unroll
  for (int r = 0; r < 4; ++r)
#pragma unroll
    for (int o = 0; o < 2; ++o) {
      if constexpr (P == 0) {
        *(f32x4*)(xb + (r * 4 + o * 2) * 1024) = s.acc[0][4 + r][o] + s.acc[1][4 + r][o];
        *(f32x4*)(xb + (r * 4 + o * 2 + 1) * 1024) = s.acc[1][4 + r][o];
      } else {
        *(f32x4*)(xb + (r * 4 + o * 2) * 1024) = s.acc[0][r][o];
        *(f32x4*)(xb + (r * 4 + o * 2 + 1) * 1024) = s.acc[0][r][o] + s.acc[1][r][o];
      }
    }
  lds_bar();
  f32x4 r0[4][2], r1[4][2];
#pragma unroll
  for (int r = 0; r < 4; ++r)
#pragma unroll
    for (int o = 0; o < 2; ++o) { r0[r][o] = *(const f32x4*)(rb + (r * 4 + o * 2) * 1024); r1[r][o] = *(const f32x4*)(rb + (r * 4 + o * 2 + 1) * 1024); }
#pragma unroll
  for (int r = 0; r < 4; ++r)
#pragma unroll
    for (int o = 0; o < 2; ++o) {
      if constexpr (P == 0) { ye[r][o] = s.acc[0][r][o] + s.acc[1][r][o] + r0[r][o]; yo[r][o] = sub4(s.acc[1][r][o], r1[r][o]); }
      else                  { ye[r][o] = r0[r][o] + s.acc[0][4 + r][o]; yo[r][o] = sub4(r1[r][o], s.acc[0][4 + r][o] + s.acc[1][4 + r][o]); }
    }
}

DI void load_affine(Affine& af, const char* lds, int ho, int kg) {
  const float* red = (const float*)(lds + M_RED);
  const float mean = red[16], rstd = red[17];
  const f32x4 g0 = *(const f32x4*)(lds + M_GAM + (32 * ho + 8 * kg) * 4), g1 = *(const f32x4*)(lds + M_GAM + (32 * ho + 8 * kg) * 4 + 16);
  const f32x4 b0 = *(const f32x4*)(lds + M_BET + (32 * ho + 8 * kg) * 4), b1 = *(const f32x4*)(lds + M_BET + (32 * ho + 8 * kg) * 4 + 16);
#pragma unroll
  for (int q = 0; q < 4; ++q) {
    const f32x2 g = q == 0 ? lo2(g0) : q == 1 ? hi2(g0) : q == 2 ? lo2(g1) : hi2(g1);
    const f32x2 bb = q == 0 ? lo2(b0) : q == 1 ? hi2(b0) : q == 2 ? lo2(b1) : hi2(b1);
    af.sa[q] = g * rstd; af.sb[q] = bb - af.sa[q] * mean;
    af.sc[q] = af.sa[q] * -1.44269504088896341f; af.sd[q] = af.sb[q] * -1.44269504088896341f;
  }
}
DI void set_tag(char* ws, int bid, int word, unsigned ep, int lane) {
  if (lane == 0) __hip_atomic_store((gu32*)(ws + OFF_LINES) + bid * 32 + word, ep, RLX_AGENT);
}
template <int NQ> DI void wait_tags(char* ws, int who, int w0, bool need, unsigned ep) {
  const __amdgpu_buffer_rsrc_t rl = __builtin_amdgcn_make_buffer_rsrc((void*)(ws + OFF_LINES), (short)0, 256 * 128, 0x00020000);
  for (unsigned spins = 0;; ++spins) {
    bool ok = true;
#pragma unroll
    for (int q = 0; q < NQ; ++q) {
      const u32x4 v = __builtin_amdgcn_raw_buffer_load_b128(rl, who * 128 + w0 * 4 + q * 16, 0, 16);
      ok = ok && v[0] == ep && v[1] == ep && v[2] == ep && v[3] == ep;
    }
    if (__all(!need || ok)) break;
    if (spins > SPIN_CAP) break;
    __builtin_amdgcn_s_sleep(2);
    asm volatile("" ::: "memory");
  }
  asm volatile("" ::: "memory");
}
DI unsigned lds_key(unsigned ep, int idx) { return (ep * 16u + (unsigned)idx) * 0x9E3779B1u + 0x3C6EF372u; }
DI void lds_flag_set(char* lds, int idx, unsigned ep) {
  const unsigned key = lds_key(ep, idx);
  __hip_atomic_store((__attribute__((address_space(3))) unsigned*)(lds + M_RED + 96 + idx * 4), key, __ATOMIC_RELAXED, __HIP_MEMORY_SCOPE_WORKGROUP);
}
DI void lds_flag_wait(char* lds, int idx, unsigned ep) {
  const unsigned key = lds_key(ep, idx);
  for (unsigned spins = 0; spins < (SPIN_CAP << 4); ++spins) {
    const unsigned v = __hip_atomic_load((__attribute__((address_space(3))) unsigned*)(lds + M_RED + 96 + idx * 4), __ATOMIC_RELAXED, __HIP_MEMORY_SCOPE_WORKGROUP);
    if (__builtin_amdgcn_readfirstlane(v) == key) break;
    __builtin_amdgcn_s_sleep(1);
  }
  asm volatile("" ::: "memory");
}
DI int gran_slot(const Tile& T) { return T.ty * 4 + T.tx * 2 + (T.nt & 1); }
DI void group_stats(char* ws, int conv, const Tile& T, float tot, float m2, unsigned ep, int lane) {
  gu64* gran = (gu64*)(ws + OFF_GRAN) + (conv * 8 + T.b * 4 + (T.nt >> 1)) * 64;
  if (lane < 2) {
    const float val = lane == 0 ? tot : m2;
    __hip_atomic_store(gran + gran_slot(T) * 2 + lane, ((unsigned long long)ep << 32) | (unsigned long long)__float_as_uint(val), RLX_AGENT);
  }
}
DI void group_stats_wait(char* ws, int conv, const Tile& T, float tot, float m2, unsigned ep, float* red, int lane) {
  gu64* gran = (gu64*)(ws + OFF_GRAN) + (conv * 8 + T.b * 4 + (T.nt >> 1)) * 64;
  const bool own = (lane >> 1) == gran_slot(T);
  unsigned long long g;
  for (unsigned spins = 0;; ++spins) {
    g = __hip_atomic_load(gran + lane, RLX_AGENT);
    if (__all(own || (unsigned)(g >> 32) == ep)) break;
    if (spins > SPIN_CAP) break;
    __builtin_amdgcn_s_sleep(1);
  }
  const float val = own ? ((lane & 1) ? m2 : tot) : __uint_as_float((unsigned)g);
  const float qk = dpp0<0x101>(val);
  float mean, rstd;
  gn_combine(val, qk, !(lane & 1), 16384.f, mean, rstd);
  if (lane == 0) { red[16] = mean; red[17] = rstd; }
}

DI void put_row(char* lds, __amdgpu_buffer_rsrc_t rv, const Tile& T, int ho, int rho, bool publish, int i16, int kg, const u32x4 (&o)[4]) {
  const int pos = i16 * 64 + vpos(kg, i16) * 16;
  char* d = lds + 2 * ho * VSLOT + rho * 1024 + pos;
#pragma unroll
  for (int xi = 0; xi < 4; ++xi) *(u32x4*)(d + (xi & 1) * VSLOT + (xi >> 1) * 10240) = o[xi];
  if (publish) {
    const int so = (T.b * 16 + 2 * T.nt + ho) * 524288 + (T.y0 - 1 + rho) * 2048 + T.tx * 1024;
#pragma unroll
    for (int xi = 0; xi < 4; ++xi)
      __builtin_amdgcn_raw_buffer_store_b128(o[xi], rv, pos + so + (xi & 1) * 262144 + (xi >> 1) * 131072, 0, V_STORE_AUX);
  }
}

template <int NR> struct XRows { unsigned v[NR][8]; unsigned short col[2]; u32x4 gv, bv; unsigned short gc, bc; };
DI int clamp_y(int y) { return min(max(y, 0), 63); }
template <int NR> DI void rows_stage(const int (&rho)[NR], int k0, int k1, unsigned ldsb, const u16* __restrict__ x, const Tile& T, int ho, int lane) {
  const __amdgpu_buffer_rsrc_t rx = __builtin_amdgcn_make_buffer_rsrc((void*)x, (short)0, 2 * NCH * HW * 2, 0x00020000);
  const int cl = lane >> 3;
#pragma unroll
  for (int k = 0; k < NR; ++k)
#pragma unroll
    for (int g = 0; g < 4; ++g) {
      if (k < k0 || k >= k1) continue;
      const unsigned vo = (unsigned)(cl * (HW * 2) + (((lane & 7) - 4 * (g & 1)) & 7) * 16);
      const unsigned so = (unsigned)(((T.b * NCH + T.nt * 64 + 32 * ho + 8 * g) * HW + clamp_y(T.y0 - 1 + rho[k]) * 64) * 2);
      dma1(ldsb + (rho[k] * 2 + ho) * 4096 + g * 1024, vo, rx, so);
    }
}
template <int NR> DI void rows_params(XRows<NR>& X, const u16* __restrict__ gam, const u16* __restrict__ bet, const Tile& T, int ho, int lane) {
  const int kg = lane >> 4, chl = lane & 31, ch = T.nt * 64 + 32 * ho;
  X.gv = *(const u32x4*)(gam + ch + 8 * kg); X.bv = *(const u32x4*)(bet + ch + 8 * kg);
  X.gc = gam[ch + chl]; X.bc = bet[ch + chl];
}
template <int NR> DI void rows_fetch(XRows<NR>& X, const int (&rho)[NR], int k0, int k1, const char* lds, const Tile& T, int ho, int lane) {
  const int i16 = lane & 15, kg = lane >> 4;
  const int pos = (((T.tx * 4 + (i16 >> 2)) + 4 * (kg & 1)) & 7) * 16 + (i16 & 3) * 4;
#pragma unroll
  for (int k = 0; k < NR; ++k)
#pragma unroll
    for (int j = 0; j < 8; ++j)
      if (k >= k0 && k < k1) X.v[k][j] = *(const unsigned*)(lds + (rho[k] * 2 + ho) * 4096 + (8 * kg + j) * 128 + pos);
}
template <int NR> DI void rows_fetch_col(XRows<NR>& X, int rc0, int rc1, const char* lds, const Tile& T, int ho, int lane) {
  const int chl = lane & 31;
  const int xc = T.tx == 0 ? 32 : 31;
  const int cpos = chl * 128 + (((xc >> 3) + 4 * ((chl >> 3) & 1)) & 7) * 16 + (xc & 7) * 2;
  X.col[0] = *(const unsigned short*)(lds + (rc0 * 2 + ho) * 4096 + cpos);
  X.col[1] = *(const unsigned short*)(lds + (rc1 * 2 + ho) * 4096 + cpos);
}
template <int NR, int NI> DI void rows_stats(const XRows<NR>& X, float* red, int wid, int lane) {
  const float piv = __builtin_bit_cast(float, __builtin_amdgcn_readfirstlane(X.v[0][0] << 16));
  f32x2 sv = {0.f, 0.f}, qv = {0.f, 0.f};
#pragma unroll
  for (int k = 0; k < NI; ++k)
#pragma unroll
    for (int j = 0; j < 8; ++j) {
      const f32x2 d = f32x2{bf_lo(X.v[k][j]), bf_hi(X.v[k][j])} - piv;
      sv += d; qv = __builtin_elementwise_fma(d, d, qv);
    }
  const float s = wave_sum(sv[0] + sv[1]), q = wave_sum(qv[0] + qv[1]);
  if (lane == 0) { red[wid] = s + piv * (float)(NI * 1024); red[8 + wid] = q - s * s * (1.f / (float)(NI * 1024)); }
}
template <int NR> DI void rows_emit(const XRows<NR>& X, const int (&rho)[NR], int k0, int k1, char* lds, char* ws, const Tile& T, int wid, int ho, int lane) {
  const int i16 = lane & 15, kg = lane >> 4;
  const float* red = (const float*)(lds + M_RED);
  const float mean = red[16], rstd = red[17];
  float sa[8], sb[8];
#pragma unroll
  for (int j = 0; j < 8; ++j) {
    sa[j] = rstd * ((j & 1) ? bf_hi(X.gv[j >> 1]) : bf_lo(X.gv[j >> 1]));
    sb[j] = ((j & 1) ? bf_hi(X.bv[j >> 1]) : bf_lo(X.bv[j >> 1])) - mean * sa[j];
  }
  const float* col = (const float*)(lds + M_COL + wid * 512);
  const __amdgpu_buffer_rsrc_t rv = __builtin_amdgcn_make_buffer_rsrc((void*)(ws + OFF_V1), (short)0, (int)V_BYTES, 0x00020000);
#pragma unroll
  for (int k = 0; k < NR; ++k) {
    if (k < k0 || k >= k1) continue;
    f32x4 hl[2], hr[2];
    halo_ops(lds, col + k * 32, T.tx, i16, kg, hl, hr);
    float t[4][8];
#pragma unroll
    for (int j = 0; j < 8; ++j) {
      const f32x2 a2 = {sa[j], sa[j]}, b2 = {sb[j], sb[j]};
      const f32x2 d = silu_aff2(f32x2{bf_lo(X.v[k][j]), bf_hi(X.v[k][j])}, a2, b2, a2 * -1.44269504088896341f, b2 * -1.44269504088896341f);
      t[0][j] = dpp_prev(d[1]) - (d[1] - hl[j >> 2][j & 3]);
      t[1][j] = d[0] + d[1];
      t[2][j] = d[1] - d[0];
      t[3][j] = (d[0] - hr[j >> 2][j & 3]) - dpp_next(d[0]);
    }
    u32x4 o[4];
#pragma unroll
    for (int xi = 0; xi < 4; ++xi)
      o[xi] = u32x4{pack_bf16(t[xi][0], t[xi][1]), pack_bf16(t[xi][2], t[xi][3]), pack_bf16(t[xi][4], t[xi][5]), pack_bf16(t[xi][6], t[xi][7])};
    const int y = T.y0 - 1 + rho[k];
    const bool halo_row = NR == 2 && k == 1;
    if (halo_row && (y < 0 || y > 63)) { o[0] = o[1] = o[2] = o[3] = u32x4{0u, 0u, 0u, 0u}; }
    put_row(lds, rv, T, ho, rho[k], !halo_row, i16, kg, o);
  }
}
template <int NR> DI void rows_column(const XRows<NR>& X, char* lds, int wid, int lane) {
  const int chl = lane & 31, half = lane >> 5;
  const float* red = (const float*)(lds + M_RED);
  const float mean = red[16], rstd = red[17];
  float* col = (float*)(lds + M_COL + wid * 512);
  const float sa = rstd * __uint_as_float((unsigned)X.gc << 16), sb = __uint_as_float((unsigned)X.bc << 16) - mean * sa;
  col[lane] = silu_f(__uint_as_float((unsigned)X.col[0] << 16) * sa + sb);
  if constexpr (NR >= 3) {
    const float v2 = silu_f(__uint_as_float((unsigned)X.col[1] << 16) * sa + sb);
    if (half == 0) col[64 + chl] = v2;
  }
}

DI int u_row(int conv, int of, int i) { return conv == 0 ? (32 * (of >> 1) + 8 * (i >> 2) + 4 * (of & 1) + (i & 3)) : (16 * of + i); }
DI void u_load(u32x4 (&d)[9], const u16* __restrict__ w, int conv, const Tile& T, int uidx, int of, int lane) {
  const int i = lane & 15, kg = lane >> 4;
  const u16* src = w + ((size_t)(T.nt * 64 + u_row(conv, of, i)) * 512 + 32 * (uidx >> 1) + 8 * kg) * 9;
  asm volatile("global_load_dwordx4 %0, %9, off\n\tglobal_load_dwordx4 %1, %9, off offset:16\n\tglobal_load_dwordx4 %2, %9, off offset:32\n\t"
               "global_load_dwordx4 %3, %9, off offset:48\n\tglobal_load_dwordx4 %4, %9, off offset:64\n\tglobal_load_dwordx4 %5, %9, off offset:80\n\t"
               "global_load_dwordx4 %6, %9, off offset:96\n\tglobal_load_dwordx4 %7, %9, off offset:112\n\tglobal_load_dwordx4 %8, %9, off offset:128"
               : "=&v"(d[0]), "=&v"(d[1]), "=&v"(d[2]), "=&v"(d[3]), "=&v"(d[4]), "=&v"(d[5]), "=&v"(d[6]), "=&v"(d[7]), "=&v"(d[8])
               : "v"(src) : "memory");
}
DI void u_landed(u32x4 (&d)[9]) {
  asm volatile("" : "+v"(d[0]), "+v"(d[1]), "+v"(d[2]), "+v"(d[3]), "+v"(d[4]), "+v"(d[5]), "+v"(d[6]), "+v"(d[7]), "+v"(d[8]) :: "memory");
}
DI void u_stage(unsigned ldsdst, const u16* __restrict__ w, int conv, const Tile& T, int uidx, int of, int lane) {
  const __amdgpu_buffer_rsrc_t rw = __builtin_amdgcn_make_buffer_rsrc((void*)w, (short)0, 512 * 512 * 9 * 2, 0x00020000);
#pragma unroll
  for (int k = 0; k < 9; ++k) {
    const int t = k * 64 + lane, row = t / 36, c16 = t - row * 36;
    dma1(ldsdst + k * 1024, (unsigned)(((T.nt * 64 + u_row(conv, of, row)) * 512 + 32 * (uidx >> 1)) * 18 + c16 * 16), rw, 0u);
  }
}
DI void u_touch(unsigned ldsb, const u16* __restrict__ w, int conv, const Tile& T, int uidx, int of, int lane) {
  const __amdgpu_buffer_rsrc_t rw = __builtin_amdgcn_make_buffer_rsrc((void*)w, (short)0, 512 * 512 * 9 * 2, 0x00020000);
#pragma unroll
  for (int h = 0; h < 2; ++h) {
    const int t = min(64 * h + lane, 79), row = t / 5;
    const unsigned vo = (unsigned)(((T.nt * 64 + u_row(conv, of, row)) * 512 + 32 * (uidx >> 1)) * 18 + (t - row * 5) * 128);
    asm volatile("s_mov_b32 m0, %0\n\ts_nop 0\n\tbuffer_load_dword %1, %2, 0 offen lds" :: "s"(ldsb + M_DUMP), "v"(vo), "s"(rw) : "memory");
  }
}
DI void u_fetch(u32x4 (&d)[9], const char* stage, int lane) {
#pragma unroll
  for (int k = 0; k < 9; ++k) d[k] = *(const u32x4*)(stage + (lane & 15) * 576 + (lane >> 4) * 144 + k * 16);
}
DI void u_emit(const u32x4 (&d)[9], char* ws, int conv, const Tile& T, int uidx, int of, int lane) {
  const __amdgpu_buffer_rsrc_t ru = __builtin_amdgcn_make_buffer_rsrc((void*)(ws + (conv == 0 ? OFF_U1 : OFF_U2)), (short)0, (int)U_BYTES, 0x00020000);
  const int dst = T.nt * 786432 + uidx * USLOT + of * 1024 + lane * 16;
  const float sg = (uidx & 1) ? 1.f : -1.f;
#pragma unroll
  for (int dy = 0; dy < 3; ++dy) {
    float ua[8], ub[8];
#pragma unroll
    for (int j = 0; j < 8; ++j) {
      float g[3];
#pragma unroll
      for (int dx = 0; dx < 3; ++dx) {
        const int e = 9 * j + 3 * dy + dx;
        const unsigned dw = d[e >> 3][(e >> 1) & 3];
        g[dx] = (e & 1) ? bf_hi(dw) : bf_lo(dw);
      }
      const float mid = 0.5f * (g[0] + sg * g[1] + g[2]);
      ua[j] = (uidx & 1) ? mid : g[0];
      ub[j] = (uidx & 1) ? g[2] : mid;
    }
    const u32x4 oa = {pack_bf16(ua[0], ua[1]), pack_bf16(ua[2], ua[3]), pack_bf16(ua[4], ua[5]), pack_bf16(ua[6], ua[7])};
    const u32x4 ob = {pack_bf16(ub[0], ub[1]), pack_bf16(ub[2], ub[3]), pack_bf16(ub[4], ub[5]), pack_bf16(ub[6], ub[7])};
    __builtin_amdgcn_raw_buffer_store_b128(oa, ru, dst + dy * 4096, 0, V_STORE_AUX);
    __builtin_amdgcn_raw_buffer_store_b128(ob, ru, dst + 12288 + dy * 4096, 0, V_STORE_AUX);
  }
}

template <int P> DI void epi1_mfma(const Comp& s, char* lds, char* ws, const Tile& T, unsigned ep, int wid, int lane) {
  constexpr int MODE = 1; (void)MODE;
  const int i16 = lane & 15, kg = lane >> 4, ho = wid >> 1;
  f32x4 ye[4][2], yo[4][2];
  exchange<P>(s, lds, wid, lane, ye, yo);
  if (wid == 0) { STAMP(12); }
  {
    const f32x4 e0 = *(const f32x4*)(lds + M_EMB + (32 * ho + 8 * kg) * 4), e1 = *(const f32x4*)(lds + M_EMB + (32 * ho + 8 * kg) * 4 + 16);
#pragma unroll
    for (int r = 0; r < 4; ++r) { ye[r][0] += e0; ye[r][1] += e1; yo[r][0] += e0; yo[r][1] += e1; }
  }
  const __amdgpu_buffer_rsrc_t rh = __builtin_amdgcn_make_buffer_rsrc((void*)(ws + OFF_HALO), (short)0, 256 * HALO_STRIDE, 0x00020000);
  float* red = (float*)(lds + M_RED);
  const int hb = T.bid * HALO_STRIDE;
  constexpr int RE = P == 0 ? 0 : 3;
#pragma unroll
  for (int f = 0; f < 2; ++f) {
    __builtin_amdgcn_raw_buffer_store_b128(__builtin_bit_cast(u32x4, ye[RE][f]), rh, hb + P * 8192 + ((ho * 2 + f) * 2) * 1024 + lane * 16, 0, 16);
    __builtin_amdgcn_raw_buffer_store_b128(__builtin_bit_cast(u32x4, yo[RE][f]), rh, hb + P * 8192 + ((ho * 2 + f) * 2 + 1) * 1024 + lane * 16, 0, 16);
  }
  if (i16 == (T.tx == 0 ? 15 : 0)) {
#pragma unroll
    for (int r = 0; r < 4; ++r)
#pragma unroll
      for (int f = 0; f < 2; ++f)
        __builtin_amdgcn_raw_buffer_store_b128(__builtin_bit_cast(u32x4, T.tx == 0 ? yo[r][f] : ye[r][f]), rh,
                                               hb + 16384 + (4 * P + r) * 256 + (32 * ho + 8 * kg + 4 * f) * 4, 0, 16);
  }
  {
    const float piv = __builtin_bit_cast(float, __builtin_amdgcn_readfirstlane(__builtin_bit_cast(int, ye[0][0][0])));
    f32x4 sv = {0.f, 0.f, 0.f, 0.f}, qv = {0.f, 0.f, 0.f, 0.f};
#pragma unroll
    for (int r = 0; r < 4; ++r)
#pragma unroll
      for (int f = 0; f < 2; ++f) {
        const f32x4 d0 = ye[r][f] - piv, d1 = yo[r][f] - piv;
        sv += d0 + d1;
        qv = __builtin_elementwise_fma(d0, d0, qv);
        qv = __builtin_elementwise_fma(d1, d1, qv);
      }
    const float sum = wave_sum((sv[0] + sv[1]) + (sv[2] + sv[3])), q = wave_sum((qv[0] + qv[1]) + (qv[2] + qv[3]));
    if (lane == 0) { red[wid] = sum + piv * 4096.f; red[8 + wid] = q - sum * sum * (1.f / 4096.f); }
  }
  constexpr int RH = P == 0 ? 3 : 0;
  char* hd = lds + X_HAND + wid * 4096 + lane * 16;
#pragma unroll
  for (int f = 0; f < 2; ++f) { *(f32x4*)(hd + (f * 2) * 1024) = ye[RH][f]; *(f32x4*)(hd + (f * 2 + 1) * 1024) = yo[RH][f]; }
  lds_bar();
  if (wid == 0) { STAMP(13); }
  wait_vm<0>();
  set_tag(ws, T.bid, W_HALO + wid, ep, lane);
  const int chl = lane & 31, half = lane >> 5;
  const float gam_c = ((const float*)(lds + M_GAM))[32 * ho + chl], bet_c = ((const float*)(lds + M_BET))[32 * ho + chl];
  lds_bar();
  if (wid == 0) { STAMP(11); }
  Affine af;
  load_affine(af, lds, ho, kg);
  float* col = (float*)(lds + M_COL + wid * 512);
  {
    const float* raw = (const float*)(lds + M_RAW);
    const float sa = red[17] * gam_c, sb = bet_c - red[16] * sa;
    col[lane] = silu_f(raw[(5 * P + 1 + half) * 64 + 32 * ho + chl] * sa + sb);
    const float v2 = silu_f(raw[(5 * P + 3) * 64 + 32 * ho + chl] * sa + sb);
    if (half == 0) col[64 + chl] = v2;
  }
  if (wid == 0) { STAMP(18); }
  const __amdgpu_buffer_rsrc_t rv = __builtin_amdgcn_make_buffer_rsrc((void*)(ws + OFF_V2), (short)0, (int)V_BYTES, 0x00020000);
#pragma unroll
  for (int k = 0; k < 3; ++k) {
    constexpr int R0 = P == 0 ? 0 : 1;
    f32x4 hl[2], hr[2];
    halo_ops(lds, col + k * 32, T.tx, i16, kg, hl, hr);
    u32x4 o[4];
    row_transform(ye[R0 + k], yo[R0 + k], hl, hr, af, o);
    put_row(lds, rv, T, ho, 4 * P + R0 + k + 1, true, i16, kg, o);
  }
  if (wid == 0) { STAMP(19); }
}

DI void epi1_loader(const Loader& L, const u16* __restrict__ w2, int uprod, int uidx, char* lds, char* ws, const Tile& T, unsigned ep, int wid, int lane) {
  constexpr int MODE = 1; (void)MODE;
  const int i16 = lane & 15, kg = lane >> 4, p = wid & 1, ho = (wid >> 1) & 1;
  const __amdgpu_buffer_rsrc_t rh = __builtin_amdgcn_make_buffer_rsrc((void*)(ws + OFF_HALO), (short)0, 256 * HALO_STRIDE, 0x00020000);
  float* red = (float*)(lds + M_RED);
  {
    u32x4 dw2[9];
    u_load(dw2, w2, 1, T, uidx, wid - 4, lane);
    lds_bar();
    bar();
    wait_vm<0>();
    u_landed(dw2);
    u_emit(dw2, ws, 1, T, uidx, wid - 4, lane);
  }
  bar();
  float tot = 0.f, m2 = 0.f;
  if (wid == 4) {
    const float s0 = red[0], s1 = red[1], s2 = red[2], s3 = red[3];
    tot = (s0 + s1) + (s2 + s3);
    const float bm = tot * (1.f / 16384.f);
    const float d0 = s0 * (1.f / 4096.f) - bm, d1 = s1 * (1.f / 4096.f) - bm, d2 = s2 * (1.f / 4096.f) - bm, d3 = s3 * (1.f / 4096.f) - bm;
    m2 = (red[8] + red[9]) + (red[10] + red[11]) + 4096.f * ((d0 * d0 + d1 * d1) + (d2 * d2 + d3 * d3));
    group_stats(ws, 1, T, tot, m2, ep, lane);
    STAMP(14); STAMPR(16);
  }
  wait_vm<0>();
  set_tag(ws, T.bid, W_U2 + (wid - 4), ep, lane);
  if (wid == 4) {
    group_stats_wait(ws, 1, T, tot, m2, ep, red, lane);
    STAMP(15); STAMPR(17);
  }
  const int tyn = p == 0 ? T.ty - 1 : T.ty + 1;
  const bool have_v = tyn >= 0 && tyn < 8;
  const int bid_v = tile_bid(T.b, have_v ? tyn : T.ty, T.tx, T.nt);
  f32x4 ve[2], vo[2];
  const int hbv = bid_v * HALO_STRIDE + (p == 0 ? 8192 : 0) + ho * 4096 + lane * 16;
  if (wid != 4) {
    wait_tags<1>(ws, bid_v, W_HALO, have_v, ep);
#pragma unroll
    for (int f = 0; f < 2; ++f) {
      ve[f] = __builtin_bit_cast(f32x4, __builtin_amdgcn_raw_buffer_load_b128(rh, hbv + f * 2048, 0, 16));
      vo[f] = __builtin_bit_cast(f32x4, __builtin_amdgcn_raw_buffer_load_b128(rh, hbv + f * 2048 + 1024, 0, 16));
    }
  }
  if (wid == 5) {
    const int nb = T.bid ^ 32;
    wait_tags<1>(ws, nb, W_HALO, true, ep);
    const u32x4 c0 = __builtin_amdgcn_raw_buffer_load_b128(rh, nb * HALO_STRIDE + 16384 + lane * 16, 0, 16);
    const u32x4 c1 = __builtin_amdgcn_raw_buffer_load_b128(rh, nb * HALO_STRIDE + 16384 + 1024 + lane * 16, 0, 16);
    *(u32x4*)(lds + M_RAW + 256 + lane * 16) = c0;
    *(u32x4*)(lds + M_RAW + 256 + 1024 + lane * 16) = c1;
  } else if (wid >= 6) {
    const int tyd = wid == 6 ? T.ty - 1 : T.ty + 1;
    float cv = 0.f;
    if (tyd >= 0 && tyd < 8) {
      const int nb = tile_bid(T.b, tyd, T.tx ^ 1, T.nt);
      wait_tags<1>(ws, nb, W_HALO, true, ep);
      cv = __uint_as_float(__builtin_amdgcn_raw_buffer_load_b32(rh, nb * HALO_STRIDE + 16384 + (wid == 6 ? 7 : 0) * 256 + lane * 4, 0, 16));
    }
    ((float*)(lds + M_RAW))[(wid == 6 ? 0 : 9) * 64 + lane] = cv;
  }
  const int chl = lane & 31, half = lane >> 5;
  const float gam_c = ((const float*)(lds + M_GAM))[32 * ho + chl], bet_c = ((const float*)(lds + M_BET))[32 * ho + chl];
  f32x4 he[2], hodd[2];
  const char* hd = lds + X_HAND + (wid - 4) * 4096 + lane * 16;
#pragma unroll
  for (int f = 0; f < 2; ++f) { he[f] = *(const f32x4*)(hd + (f * 2) * 1024); hodd[f] = *(const f32x4*)(hd + (f * 2 + 1) * 1024); }
  lds_bar();
  if (wid == 4) {
    wait_tags<1>(ws, bid_v, W_HALO, have_v, ep);
#pragma unroll
    for (int f = 0; f < 2; ++f) {
      ve[f] = __builtin_bit_cast(f32x4, __builtin_amdgcn_raw_buffer_load_b128(rh, hbv + f * 2048, 0, 16));
      vo[f] = __builtin_bit_cast(f32x4, __builtin_amdgcn_raw_buffer_load_b128(rh, hbv + f * 2048 + 1024, 0, 16));
    }
  }
  Affine af;
  load_affine(af, lds, ho, kg);
  float* col = (float*)(lds + M_COL + wid * 512);
  {
    const float* raw = (const float*)(lds + M_RAW);
    const float sa = red[17] * gam_c, sb = bet_c - red[16] * sa;
    col[lane] = silu_f(raw[(half == 0 ? 4 + p : 9 * p) * 64 + 32 * ho + chl] * sa + sb);
  }
  const __amdgpu_buffer_rsrc_t rv = __builtin_amdgcn_make_buffer_rsrc((void*)(ws + OFF_V2), (short)0, (int)V_BYTES, 0x00020000);
  f32x4 hl[2], hr[2];
  u32x4 o[4];
  if (wid == 5) {
    wait_tags<1>(ws, uprod, W_U2, lane < 32 && lane != uidx, ep);
    lds_flag_set(lds, 3, ep);
    ld_u(L, 0); ld_u(L, 1); ld_u(L, 2);
  }
  halo_ops(lds, col, T.tx, i16, kg, hl, hr);
  row_transform(he, hodd, hl, hr, af, o);
  put_row(lds, rv, T, ho, 4 + p, true, i16, kg, o);
  asm volatile("" : "+v"(ve[0]), "+v"(ve[1]), "+v"(vo[0]), "+v"(vo[1]));
  if (wid != 5) {
    lds_flag_wait(lds, 3, ep);
    ld_u(L, 0); ld_u(L, 1); ld_u(L, 2);
  }
  halo_ops(lds, col + 32, T.tx, i16, kg, hl, hr);
  row_transform(ve, vo, hl, hr, af, o);
  if (!have_v) { o[0] = o[1] = o[2] = o[3] = u32x4{0u, 0u, 0u, 0u}; }
  put_row(lds, rv, T, ho, 9 * p, false, i16, kg, o);
  if (wid == 5) wait_vm<4>(); else wait_vm<0>();
}

constexpr int xpiece(int k) { return k < 24 ? LDS_U + 2 * USLOT + k * 1024 : LDS_U + (k - 24) * 1024; }

template <int P> DI void epi2_mfma(const Comp& s, char* lds, char* ws, const Tile& T, u16* __restrict__ out, int wid, int lane) {
  constexpr int MODE = 2; (void)MODE; (void)ws;
  const int i16 = lane & 15, kg = lane >> 4, ho = wid >> 1;
  bar();
  f32x4 ye[4][2], yo[4][2];
  exchange<P>(s, lds, wid, lane, ye, yo);
  if (wid == 0) { STAMP(12); }
  const size_t off0 = ((size_t)(T.b * NCH + T.nt * 64 + 32 * ho + i16)) * HW + (T.y0 + 4 * P) * 64 + 32 * T.tx + 8 * kg;
  [[maybe_unused]] const __amdgpu_buffer_rsrc_t ro = __builtin_amdgcn_make_buffer_rsrc((void*)out, (short)0, 2 * NCH * HW * 2, 0x00020000);
#pragma unroll
  for (int f = 0; f < 2; ++f)
#pragma unroll
    for (int r = 0; r < 4; ++r) {
      const int k = (ho * 2 + f) * 8 + 4 * P + r;
      const u32x4 xv = *(const u32x4*)(lds + (k < 24 ? LDS_U + 2 * USLOT + k * 1024 : LDS_U + (k - 24) * 1024) + lane * 16);
      u32x4 o;
#pragma unroll
      for (int j = 0; j < 4; ++j) o[j] = pack_bf16(ye[r][f][j] + bf_lo(xv[j]), yo[r][f][j] + bf_hi(xv[j]));
      __builtin_amdgcn_raw_buffer_store_b128(o, ro, (int)((off0 + (size_t)f * 16 * HW + r * 64) * 2), 0, OUT_AUX);
    }
}

template <int MODE> DI void loader_loop(const Loader& L, int uidx, int uprod, u32x4 (&ep8)[8], char* lds, char* ws, const Tile& T, unsigned ep, int w, int lane) {
  const int pty = T.ty - 1 + (lane >> 3);
  const bool need = lane < 24 && pty >= 0 && pty < 8 && (lane & 7) != T.nt;
  const int pbid = need ? tile_bid(T.b, pty, T.tx, lane & 7) : T.bid;
  constexpr int WV = MODE == 1 ? W_V1 : W_V2;
  lds_bar();
  if (w == 0) { STAMPR(20); }
  u32x4 tg0 = {0u, 0u, 0u, 0u}, tg1 = {0u, 0u, 0u, 0u};
  if (w == 0) {
    const char* tp = ws + OFF_LINES + pbid * 128 + WV * 4;
    asm volatile("global_load_dwordx4 %0, %2, off sc1\n\tglobal_load_dwordx4 %1, %2, off offset:16 sc1" : "=&v"(tg0), "=&v"(tg1) : "v"(tp) : "memory");
  }
  bar();
  ld_u(L, 3);
  if (w == 0) wait_vm<8>(); else wait_vm<6>();
  set_tag(ws, T.bid, WV + 4 + w, ep, lane);
  if (MODE == 1 && w == 3) {
    const __amdgpu_buffer_rsrc_t re = __builtin_amdgcn_make_buffer_rsrc((void*)(ws + OFF_EMBP), (short)0, 8 * 32 * 512, 0x00020000);
#pragma unroll
    for (int i = 0; i < 8; ++i) ep8[i] = __builtin_amdgcn_raw_buffer_load_b128(re, (T.nt * 32 + (lane >> 4) * 8 + i) * 512 + T.b * 256 + (lane & 15) * 16, 0, 16);
  }
  bar();
  ld_u(L, 4);
  bool early = false;
  if (w == 0) {
    wait_vm<13>();
    asm volatile("" : "+v"(tg0), "+v"(tg1) :: "memory");
    early = __all(!need || (tg0[0] == ep && tg0[1] == ep && tg0[2] == ep && tg0[3] == ep && tg1[0] == ep && tg1[1] == ep && tg1[2] == ep && tg1[3] == ep));
    if (early) lds_flag_set(lds, MODE, ep);
  }
  wait_vm<7>();
  bar();
  if (w == 0) {
    STAMPR(22);
    if (!early) { wait_tags<2>(ws, pbid, WV, need, ep); lds_flag_set(lds, MODE, ep); }
    STAMPR(23);
  } else {
    lds_flag_wait(lds, MODE, ep);
  }
  ld_v(L, 4); ld_v(L, 5); ld_u(L, 5);
  wait_vm<11>();
  bar();
#pragma unroll 1
  for (int t = 4; t < NUNIT - 2; ++t) {
    ld_v(L, t + 2); ld_u(L, t + 2);
    wait_vm<11>();
    bar();
  }
}

template <int MODE> DI void mfma_loop(Comp& s, char* ws, const u16* __restrict__ w2, const Tile& T, unsigned ldsb, unsigned ep, int wid, int lane) {
  const int bid = T.bid;
  lds_bar();
  lds_rd<0>(s.B[0][0], s.bbase); lds_rd<1024>(s.B[0][1], s.bbase); lds_rd<0>(s.A[0], s.abase);
  lds_rd<4096>(s.B[0][2], s.bbase); lds_rd<5120>(s.B[0][3], s.bbase); lds_rd<8192>(s.B[0][4], s.bbase); lds_rd<9216>(s.B[0][5], s.bbase);
  lds_rd<1024>(s.A[1], s.abase); lds_rd<2048>(s.A[2], s.abase); lds_rd<3072>(s.A[3], s.abase);
  wait_lgkm<3>();
  s.anxt = s.abase;
  unsigned voff = 0, uoff = 0;
  unit<MODE, 0, true>(s, voff, uoff);
  wait_vm<0>();
  set_tag(ws, bid, (MODE == 1 ? W_V1 : W_V2) + wid, ep, lane);
  unit<MODE, 1, true>(s, voff, uoff);
#pragma unroll 1
  for (int c = 1; c < 16; ++c) {
    unit<MODE, 0>(s, voff, uoff);
    if (MODE == 1 && c == 8) u_touch(ldsb, w2, 1, T, (T.b * 8 + T.ty) * 2 + T.tx, wid, lane);
    unit<MODE, 1>(s, voff, uoff);
  }
  asm volatile("s_waitcnt lgkmcnt(0)\n\ts_nop 15" ::: "memory");
}
__global__ __launch_bounds__(512, 2) void k_main(const u16* __restrict__ x, const u16* __restrict__ temb, const u16* __restrict__ g1, const u16* __restrict__ be1,
                                                 const u16* __restrict__ w1, const u16* __restrict__ wemb, const u16* __restrict__ g2,
                                                 const u16* __restrict__ be2, const u16* __restrict__ w2, u16* __restrict__ out, char* __restrict__ ws) {
  __shared__ __attribute__((aligned(16))) char lds[LDS_TOTAL];
  const int tid = threadIdx.x, lane = tid & 63;
  const int wid = __builtin_amdgcn_readfirstlane(tid >> 6);
  const int bid = blockIdx.x, xcd = bid & 7, slot = bid >> 3;
  Tile T;
  T.bid = bid; T.b = xcd >> 2; T.nt = (xcd & 1) * 4 + (slot & 3); T.ty = ((xcd >> 1) & 1) * 4 + (slot >> 3); T.tx = (slot >> 2) & 1; T.y0 = T.ty * 8;
  const unsigned ldsb = (unsigned)(size_t)lds;
  const int p = wid & 1, ho = (wid >> 1) & 1;
  {
    constexpr int CODE_BYTES = 52 * 1024;
    const __amdgpu_buffer_rsrc_t rc = __builtin_amdgcn_make_buffer_rsrc((void*)__builtin_amdgcn_s_getpc(), (short)0, CODE_BYTES, 0x00020000);
    asm volatile("s_mov_b32 m0, %0\n\ts_nop 0\n\tbuffer_load_dword %1, %2, 0 offen lds" :: "s"(ldsb + M_DUMP), "v"((unsigned)tid * 128u), "s"(rc) : "memory");
  }
  const unsigned ep = __builtin_amdgcn_readfirstlane(__hip_atomic_load((gu32*)(ws + OFF_EPOCH), RLX_AGENT)) + 1u;
  float* red = (float*)(lds + M_RED);

  if (wid >= 4) {
    const int w = wid - 4;
    const int uidx = (T.b * 8 + T.ty) * 2 + T.tx;
    const int rho[2] = {4 + p, 9 * p};
    const char* stage = lds + LDS_U + w * 9216;
    XRows<2> X;
    rows_stage<2>(rho, 0, 1, ldsb, x, T, ho, lane);
    wait_vm<0>();
    rows_fetch<2>(X, rho, 0, 1, lds, T, ho, lane);
    u_stage(ldsb + LDS_U + w * 9216, w1, 0, T, uidx, w, lane);
    rows_stats<2, 1>(X, red, wid, lane);
    asm volatile("s_waitcnt lgkmcnt(0)" ::: "memory");
    lds_flag_set(lds, 4 + wid, ep);
    wait_vm<0>();
#pragma unroll
    for (int k = 0; k < 4; ++k) lds_flag_wait(lds, 4 + k, ep);
    u_stage(ldsb + LDS_U + w * 9216, w1, 0, T, uidx, w, lane);
    rows_stage<2>(rho, 1, 2, ldsb, x, T, ho, lane);
    rows_params<2>(X, g1, be1, T, ho, lane);
    u32x4 we[3];
    unsigned short te[2][3];
    if (w == 3) {
#pragma unroll
      for (int i = 0; i < 3; ++i) {
        const int k = 24 * uidx + 3 * (lane >> 3) + i;
        we[i] = *(const u32x4*)(wemb + (size_t)k * 512 + T.nt * 64 + 8 * (lane & 7));
        te[0][i] = temb[k]; te[1][i] = temb[NEMB + k];
      }
    }
    wait_vm<0>();
    rows_fetch<2>(X, rho, 1, 2, lds, T, ho, lane);
    rows_fetch_col<2>(X, (lane >> 5) ? 9 * p : 4 + p, 4 + p, lds, T, ho, lane);
    { constexpr int MODE = 1; (void)MODE; if (w == 1) { STAMPR(48); } }
    {
      u32x4 dw1[9];
      u_fetch(dw1, stage, lane);
      u_emit(dw1, ws, 0, T, uidx, w, lane);
    }
    { constexpr int MODE = 1; (void)MODE; if (w == 1) { STAMPR(50); } }
    if (w == 3) {
      float a[2][8];
#pragma unroll
      for (int j = 0; j < 8; ++j) a[0][j] = a[1][j] = 0.f;
#pragma unroll
      for (int i = 0; i < 3; ++i) {
        const float t0 = silu_f(__uint_as_float((unsigned)te[0][i] << 16)), t1 = silu_f(__uint_as_float((unsigned)te[1][i] << 16));
#pragma unroll
        for (int j = 0; j < 8; ++j) {
          const float wv = (j & 1) ? bf_hi(we[i][j >> 1]) : bf_lo(we[i][j >> 1]);
          a[0][j] += t0 * wv; a[1][j] += t1 * wv;
        }
      }
      float* sc = (float*)(lds + M_COL);
#pragma unroll
      for (int j = 0; j < 8; ++j) { sc[(lane >> 3) * 128 + (lane & 7) * 8 + j] = a[0][j]; sc[(lane >> 3) * 128 + 64 + (lane & 7) * 8 + j] = a[1][j]; }
      float e0 = 0.f, e1 = 0.f;
#pragma unroll
      for (int g = 0; g < 8; ++g) { e0 += sc[g * 128 + lane]; e1 += sc[g * 128 + 64 + lane]; }
      const __amdgpu_buffer_rsrc_t re = __builtin_amdgcn_make_buffer_rsrc((void*)(ws + OFF_EMBP), (short)0, 8 * 32 * 512, 0x00020000);
      __builtin_amdgcn_raw_buffer_store_b32(__float_as_uint(e0), re, (T.nt * 32 + uidx) * 512 + lane * 4, 0, 16);
      __builtin_amdgcn_raw_buffer_store_b32(__float_as_uint(e1), re, (T.nt * 32 + uidx) * 512 + 256 + lane * 4, 0, 16);
    }
    wait_vm<0>();
    set_tag(ws, T.bid, W_U1 + w, ep, lane);
    { constexpr int MODE = 1; (void)MODE; if (w == 1) { STAMPR(52); } }
    lds_bar();
    Loader L;
    L.nt = T.nt;
    L.rv = __builtin_amdgcn_make_buffer_rsrc((void*)(ws + OFF_V1), (short)0, (int)V_BYTES, 0x00020000);
    L.ru = __builtin_amdgcn_make_buffer_rsrc((void*)(ws + OFF_U1), (short)0, (int)U_BYTES, 0x00020000);
    L.vmid = lane * 16;
    L.vfirst = (T.y0 == 0 && !(w & 1)) ? 0x80000000u : L.vmid;
    L.vlast = (T.y0 == 56 && (w & 1)) ? 0x80000000u : L.vmid;
    L.sv = (unsigned)T.b * 16u * 524288u + (unsigned)(w >> 1) * 131072u + (unsigned)((T.y0 + 5 * (w & 1)) * 2048 + T.tx * 1024) - 2048u;
    L.su = (unsigned)T.nt * 786432u + (unsigned)w * 6144u;
    L.mv = ldsb + (w >> 1) * 10240 + (w & 1) * 5120;
    L.mu = ldsb + LDS_U + w * 6144;
    const int uprod = tile_bid((lane >> 4) & 1, (lane >> 1) & 7, lane & 1, T.nt);
    rows_column<2>(X, lds, wid, lane);
    rows_emit<2>(X, rho, 1, 2, lds, ws, T, wid, ho, lane);
    if (w == 1) {
      u32x4 tg;
      const char* tp = ws + OFF_LINES + uprod * 128 + W_U1 * 4;
      asm volatile("global_load_dwordx4 %0, %1, off sc1" : "=&v"(tg) : "v"(tp) : "memory");
      rows_emit<2>(X, rho, 0, 1, lds, ws, T, wid, ho, lane);
      wait_vm<4>();
      asm volatile("" : "+v"(tg) :: "memory");
      const bool need = lane < 32 && lane != uidx;
      if (!__all(!need || (tg[0] == ep && tg[1] == ep && tg[2] == ep && tg[3] == ep))) wait_tags<1>(ws, uprod, W_U1, need, ep);
      lds_flag_set(lds, 0, ep);
    } else {
      rows_emit<2>(X, rho, 0, 1, lds, ws, T, wid, ho, lane);
      lds_flag_wait(lds, 0, ep);
    }
    { constexpr int MODE = 1; (void)MODE; if (w == 1) { STAMPR(53); } }
    ld_u(L, 0); ld_u(L, 1); ld_u(L, 2);
    wait_vm<0>();
    u32x4 ep8[8];
    loader_loop<1>(L, uidx, uprod, ep8, lds, ws, T, ep, w, lane);
    if (w == 3) {
      f32x4 e = {0.f, 0.f, 0.f, 0.f};
#pragma unroll
      for (int i = 0; i < 8; ++i) e += __builtin_bit_cast(f32x4, ep8[i]);
#pragma unroll
      for (int j = 0; j < 4; ++j) { e[j] += __shfl_xor(e[j], 16); e[j] += __shfl_xor(e[j], 32); }
      if (lane < 16) *(f32x4*)(lds + M_EMB + lane * 16) = e;
    }
    wait_vm<0>();
    bar();
    bar();
    L.rv = __builtin_amdgcn_make_buffer_rsrc((void*)(ws + OFF_V2), (short)0, (int)V_BYTES, 0x00020000);
    L.ru = __builtin_amdgcn_make_buffer_rsrc((void*)(ws + OFF_U2), (short)0, (int)U_BYTES, 0x00020000);
    epi1_loader(L, w2, uprod, uidx, lds, ws, T, ep, wid, lane);
    loader_loop<2>(L, uidx, uprod, ep8, lds, ws, T, ep, w, lane);
    {
      const __amdgpu_buffer_rsrc_t rx = __builtin_amdgcn_make_buffer_rsrc((void*)x, (short)0, 2 * NCH * HW * 2, 0x00020000);
      const unsigned vx = (lane & 15) * (HW * 2) + (lane >> 4) * 16;
      const unsigned sx = (unsigned)((T.b * NCH + T.nt * 64) * HW + T.y0 * 64 + 32 * T.tx) * 2u;
#pragma unroll
      for (int q = 0; q < 6; ++q) { const int k = w * 6 + q; dma1(ldsb + xpiece(0) + k * 1024, vx, rx, sx + (unsigned)(k >> 3) * 16u * HW * 2u + (unsigned)(k & 7) * 128u); }
      wait_vm<6>();
      bar();
#pragma unroll
      for (int q = 0; q < 2; ++q) { const int k = 24 + w * 2 + q; dma1(ldsb + xpiece(24) + (k - 24) * 1024, vx, rx, sx + (unsigned)(k >> 3) * 16u * HW * 2u + (unsigned)(k & 7) * 128u); }
      bar();
      wait_vm<0>();
      bar();
    }
    return;
  }

  const int i16 = lane & 15, kg = lane >> 4;
  {
    constexpr int MODE = 1; (void)MODE;
    if (wid == 0) { STAMP(0); STAMPR(4); }
    const int rho[3] = {5 * p + 1, 5 * p + 2, 5 * p + 3};
    XRows<3> X;
    rows_stage<3>(rho, 0, 3, ldsb, x, T, ho, lane);
    rows_params<3>(X, g1, be1, T, ho, lane);
    unsigned short g2c = 0, b2c = 0;
    if (wid == 1) { g2c = g2[T.nt * 64 + lane]; b2c = be2[T.nt * 64 + lane]; }
    wait_vm<0>();
    rows_fetch<3>(X, rho, 0, 3, lds, T, ho, lane);
    rows_fetch_col<3>(X, 5 * p + 1 + (lane >> 5), 5 * p + 3, lds, T, ho, lane);
    rows_stats<3, 3>(X, red, wid, lane);
    if (wid == 1) {
      if (lane < 8) ((float*)(lds + M_ZERO))[lane] = 0.f;
      ((float*)(lds + M_GAM))[lane] = __uint_as_float((unsigned)g2c << 16);
      ((float*)(lds + M_BET))[lane] = __uint_as_float((unsigned)b2c << 16);
    }
    asm volatile("s_waitcnt lgkmcnt(0)" ::: "memory");
    lds_flag_set(lds, 4 + wid, ep);
    if (wid == 0) {
      STAMP(24);
#pragma unroll
      for (int k = 1; k < 8; ++k) lds_flag_wait(lds, 4 + k, ep);
      float tot = 0.f;
#pragma unroll
      for (int k = 0; k < 8; ++k) tot += red[k];
      const float bm = tot * (1.f / 16384.f);
      float m2 = 0.f;
#pragma unroll
      for (int k = 0; k < 8; ++k) {
        const float n = k < 4 ? 3072.f : 1024.f, d = red[k] * (k < 4 ? 1.f / 3072.f : 1.f / 1024.f) - bm;
        m2 += red[8 + k] + n * d * d;
      }
      group_stats(ws, 0, T, tot, m2, ep, lane);
      group_stats_wait(ws, 0, T, tot, m2, ep, red, lane);
      STAMPR(54);
    }
    lds_bar();
    if (wid == 0) { STAMP(25); }
    rows_column<3>(X, lds, wid, lane);
    rows_emit<3>(X, rho, 0, 3, lds, ws, T, wid, ho, lane);
  }
  Comp s;
  s.abase = ldsb + p * 10240 + i16 * 64 + vpos(kg, i16) * 16;
  s.bbase = ldsb + LDS_U + p * 12288 + ho * 2048 + lane * 16;
  {
    constexpr int MODE = 1; (void)MODE;
    if (wid == 0) { STAMP(1); }
    mfma_loop<1>(s, ws, w2, T, ldsb, ep, wid, lane);
    if (wid == 0) { STAMP(2); STAMPR(5); }
    bar();
    if (p == 0) epi1_mfma<0>(s, lds, ws, T, ep, wid, lane); else epi1_mfma<1>(s, lds, ws, T, ep, wid, lane);
  }
  {
    constexpr int MODE = 2; (void)MODE;
    if (wid == 0) { STAMP(0); STAMPR(4); }
      if (wid == 0) { STAMP(1); }
    mfma_loop<2>(s, ws, w2, T, ldsb, ep, wid, lane);
    if (wid == 0) { STAMP(2); STAMPR(5); }
    if (p == 0) epi2_mfma<0>(s, lds, ws, T, out, wid, lane); else epi2_mfma<1>(s, lds, ws, T, out, wid, lane);
  }
  if (wid == 0 && lane < 16) ((unsigned*)(lds + M_RED + 96))[lane] = 0u;
  if (bid == 0 && tid == 0) __hip_atomic_store((gu32*)(ws + OFF_EPOCH), ep, RLX_AGENT);
}

extern "C" void kernel_launch(void* const* d_in, const int* in_sizes, int n_in, void* d_out, int out_size, void* d_ws, size_t ws_size,
                              hipStream_t stream) {
  k_main<<<256, 512, 0, stream>>>((const u16*)d_in[0], (const u16*)d_in[1], (const u16*)d_in[2], (const u16*)d_in[3], (const u16*)d_in[4],
                                  (const u16*)d_in[5], (const u16*)d_in[6], (const u16*)d_in[7], (const u16*)d_in[8], (u16*)d_out, (char*)d_ws);
}
```

```cpp
#include <hip/hip_runtime.h>
#include <hip/hip_bf16.h>
#include <stdint.h>
#include <utility>

#define DI __device__ __forceinline__
typedef __bf16 bf16x8 __attribute__((ext_vector_type(8)));
typedef __bf16 bf16x2 __attribute__((ext_vector_type(2)));
typedef float f32x4 __attribute__((ext_vector_type(4)));
typedef float f32x2 __attribute__((ext_vector_type(2)));
typedef unsigned u32x4 __attribute__((ext_vector_type(4)));
typedef unsigned u32x2 __attribute__((ext_vector_type(2)));
typedef unsigned short u16;
#define GAS __attribute__((address_space(1)))
typedef GAS unsigned long long gu64;
typedef GAS unsigned gu32;
#define RLX_AGENT __ATOMIC_RELAXED, __HIP_MEMORY_SCOPE_AGENT

constexpr int NCH = 512, HW = 4096, NEMB = 768;
constexpr float GN_EPS = 1e-5f;
constexpr unsigned SPIN_CAP = 1u << 20;
#define CODE_BYTES (50 * 1024)
#define V_STORE_AUX 16
#define OUT_AUX 16

constexpr size_t U_BYTES = (size_t)8 * 16 * 49152;
constexpr size_t V_BYTES = (size_t)2 * 16 * 4 * 131072;
constexpr size_t OFF_U1 = 0, OFF_U2 = U_BYTES;
constexpr size_t OFF_V1 = 2 * U_BYTES, OFF_V2 = OFF_V1 + V_BYTES;
constexpr size_t OFF_EPOCH = OFF_V2 + V_BYTES;
constexpr size_t OFF_GRAN = OFF_EPOCH + 256;
constexpr size_t OFF_LINES = OFF_GRAN + 8192;
constexpr int W_HALO = 0, W_V1 = 4, W_V2 = 8, W_U1 = 12, W_U2 = 16;
constexpr size_t OFF_EMBP = OFF_LINES + 256 * 128;
constexpr size_t OFF_HALO = OFF_EMBP + 8 * 32 * 512;
constexpr int HALO_STRIDE = 18432;
constexpr size_t OFF_DIAG = OFF_HALO + 256 * HALO_STRIDE;
#define STAMP(i)
#define STAMPR(i)

DI float bf_lo(unsigned v) { return __uint_as_float(v << 16); }
DI float bf_hi(unsigned v) { return __uint_as_float(v & 0xffff0000u); }
DI unsigned pack_bf16(float lo, float hi) {
  f32x2 v = {lo, hi};
  bf16x2 b = __builtin_convertvector(v, bf16x2);
  return __builtin_bit_cast(unsigned, b);
}
DI float silu_f(float y) { return y * __builtin_amdgcn_rcpf(1.f + __builtin_amdgcn_exp2f(y * -1.44269504088896341f)); }
DI f32x2 pk_sub(f32x2 a, f32x2 b) { return __builtin_elementwise_fma(b, f32x2{-1.f, -1.f}, a); }
DI f32x2 lo2(const f32x4& v) { return __builtin_shufflevector(v, v, 0, 1); }
DI f32x2 hi2(const f32x4& v) { return __builtin_shufflevector(v, v, 2, 3); }
DI f32x4 cat2(f32x2 a, f32x2 b) { return __builtin_shufflevector(a, b, 0, 1, 2, 3); }
DI f32x4 sub4(const f32x4& a, const f32x4& b) { return cat2(pk_sub(lo2(a), lo2(b)), pk_sub(hi2(a), hi2(b))); }
DI f32x2 silu_aff2(f32x2 h, f32x2 sa, f32x2 sb) {
  const f32x2 y = __builtin_elementwise_fma(h, sa, sb), t = y * -1.44269504088896341f;
  const f32x2 d = f32x2{__builtin_amdgcn_exp2f(t[0]), __builtin_amdgcn_exp2f(t[1])} + 1.f;
  return y * f32x2{__builtin_amdgcn_rcpf(d[0]), __builtin_amdgcn_rcpf(d[1])};
}
template <int CTRL, int RM = 0xf> DI float dpp0(float v) {
  return __builtin_bit_cast(float, __builtin_amdgcn_update_dpp(0, __builtin_bit_cast(int, v), CTRL, RM, 0xf, true));
}
DI float dpp_prev(float edge, float v) {
  return __builtin_bit_cast(float, __builtin_amdgcn_update_dpp(__builtin_bit_cast(int, edge), __builtin_bit_cast(int, v), 0x111  , 0xf, 0xf, false));
}
DI float dpp_next(float edge, float v) {
  return __builtin_bit_cast(float, __builtin_amdgcn_update_dpp(__builtin_bit_cast(int, edge), __builtin_bit_cast(int, v), 0x101  , 0xf, 0xf, false));
}
DI float wave_sum(float v) {
  v += dpp0<0x111>(v); v += dpp0<0x112>(v); v += dpp0<0x114>(v); v += dpp0<0x118>(v);
  v += dpp0<0x142, 0xa>(v);
  v += dpp0<0x143, 0xc>(v);
  return __builtin_bit_cast(float, __builtin_amdgcn_readlane(__builtin_bit_cast(int, v), 63));
}
DI int vpos(int o, int xt) { return o ^ ((xt >> 1) & 3); }
DI void bar() { asm volatile("s_barrier" ::: "memory"); }
DI int fresh_lane() { int l; asm volatile("v_mbcnt_lo_u32_b32 %0, -1, 0\n\tv_mbcnt_hi_u32_b32 %0, -1, %0" : "=v"(l)); return l; }
DI void lds_bar() { asm volatile("s_waitcnt lgkmcnt(0)\n\ts_barrier" ::: "memory"); }
template <int N> DI void wait_vm() { asm volatile("s_waitcnt vmcnt(%0)" :: "i"(N) : "memory"); }

DI void gn_combine(float s, float q, bool on, float n, float& mean, float& rstd) {
  const float tot = wave_sum(on ? s : 0.f);
  mean = tot * (1.f / (32.f * n));
  const float d = s * (1.f / n) - mean;
  const float m2 = wave_sum(on ? (q + n * d * d) : 0.f);
  rstd = __builtin_amdgcn_rsqf(m2 * (1.f / (32.f * n)) + GN_EPS);
}

constexpr int VSLOT = 20480, NVSLOT = 4;
constexpr int USLOT = 24576, NUSLOT = 3;
constexpr int LDS_U = NVSLOT * VSLOT, LDS_MISC = LDS_U + NUSLOT * USLOT, LDS_TOTAL = 163840;
constexpr int NUNIT = 32;
constexpr int M_EMB = LDS_MISC, M_GAM = LDS_MISC + 256, M_BET = LDS_MISC + 512, M_RED = LDS_MISC + 768, M_ZERO = LDS_MISC + 960, M_RAW = LDS_MISC + 1024, M_COL = LDS_MISC + 3584, M_DUMP = LDS_MISC + 7680;

struct Tile { int bid, b, nt, ty, tx, y0; };
DI int tile_bid(int b, int ty, int tx, int nt) { return (((ty & 3) * 8 + tx * 4 + (nt & 3)) << 3) | (b * 4 + (ty >> 2) * 2 + (nt >> 2)); }
DI int chunk_of(int j, int nt) { return j < 2 ? 2 * nt + j : (j - 2 < 2 * nt ? j - 2 : j); }

struct Loader {
  __amdgpu_buffer_rsrc_t rv, ru;
  unsigned vfirst, vmid, vlast;
  unsigned sv, su;
  unsigned mv, mu;
  int nt;
};
DI void dma1(unsigned m0, unsigned voff, __amdgpu_buffer_rsrc_t r, unsigned soff) {
  asm volatile("s_mov_b32 m0, %0\n\ts_nop 0\n\tbuffer_load_dwordx4 %1, %2, %3 offen lds" :: "s"(m0), "v"(voff), "s"(r), "s"(soff) : "memory");
}
DI void dma1_dev(unsigned m0, unsigned voff, __amdgpu_buffer_rsrc_t r, unsigned soff) {
  asm volatile("s_mov_b32 m0, %0\n\ts_nop 0\n\tbuffer_load_dwordx4 %1, %2, %3 offen sc1 lds" :: "s"(m0), "v"(voff), "s"(r), "s"(soff) : "memory");
}
DI void ld_u(const Loader& L, int k) {
  const unsigned mu = L.mu + (unsigned)(k % NUSLOT) * USLOT, su = L.su + (unsigned)(chunk_of(k >> 1, L.nt) * 2 + (k & 1)) * USLOT;
  asm volatile("s_mov_b32 m0, %0\n\ts_nop 0\n\t"
               "buffer_load_dwordx4 %1, %2, %3 offen sc1 lds\n\t"
               "buffer_load_dwordx4 %1, %2, %3 offen offset:1024 sc1 lds\n\t"
               "buffer_load_dwordx4 %1, %2, %3 offen offset:2048 sc1 lds\n\t"
               "buffer_load_dwordx4 %1, %2, %3 offen offset:3072 sc1 lds"
               :: "s"(mu), "v"(L.vmid), "s"(L.ru), "s"(su) : "memory");
  asm volatile("s_mov_b32 m0, %0\n\ts_nop 0\n\t"
               "buffer_load_dwordx4 %1, %2, %3 offen sc1 lds\n\t"
               "buffer_load_dwordx4 %1, %2, %3 offen offset:1024 sc1 lds"
               :: "s"(mu + 4096), "v"(L.vmid), "s"(L.ru), "s"(su + 4096) : "memory");
}
DI void ld_v(const Loader& L, int k) {
  const unsigned mv = L.mv + (unsigned)(k & 3) * VSLOT, sv = L.sv + (unsigned)(chunk_of(k >> 1, L.nt) * 2 + (k & 1)) * 262144u;
  dma1_dev(mv, L.vfirst, L.rv, sv);
  dma1_dev(mv + 1024, L.vmid, L.rv, sv + 2048);
  dma1_dev(mv + 2048, L.vmid, L.rv, sv + 4096);
  dma1_dev(mv + 3072, L.vmid, L.rv, sv + 6144);
  dma1_dev(mv + 4096, L.vlast, L.rv, sv + 8192);
}

struct Comp {
  f32x4 acc[2][8][2];
  bf16x8 A[10];
  bf16x8 B[2][6];
  unsigned abase, bbase;
  unsigned acur, anxt, bnxt;
};
struct MD { int r, dy, o; };
constexpr MD mdecode(int m) {
  int n = 0;
  for (int r = 0; r < 10; ++r)
    for (int dy = 0; dy < 3; ++dy) {
      if (r - dy < 0 || r - dy > 7) continue;
      for (int o = 0; o < 2; ++o) { if (n == m) return MD{r, dy, o}; ++n; }
    }
  return MD{-1, -1, -1};
}
template <int OFF> DI void lds_rd(bf16x8& dst, unsigned addr) { asm volatile("ds_read_b128 %0, %1 offset:%2" : "=v"(dst) : "v"(addr), "i"(OFF)); }
template <int N> DI void wait_lgkm() { asm volatile("s_waitcnt lgkmcnt(%0)" :: "i"(N) : "memory"); }

constexpr int first_wait(int m) {
  switch (m) { case 2: return 3; case 6: return 3; case 12: return 4; case 18: return 5; case 24: return 6; case 30: return 7;
               case 36: return 8; case 42: return 9; case 46: return 10; default: return -1; }
}
template <int MODE, int E, bool FIRST, int M> DI void mstep(Comp& s) {
  constexpr MD d = mdecode(M);
  if constexpr (first_wait(M) >= 0) wait_lgkm<first_wait(M)>();
  if constexpr (FIRST && d.dy == 0) {
    if constexpr (MODE == 1) asm volatile("v_mfma_f32_16x16x32_bf16 %0, %1, %2, 0" : "=&v"(s.acc[E][d.r][d.o]) : "v"(s.B[E][d.o]), "v"(s.A[d.r]));
    else                     asm volatile("v_mfma_f32_16x16x32_bf16 %0, %1, %2, 0" : "=&v"(s.acc[E][d.r][d.o]) : "v"(s.A[d.r]), "v"(s.B[E][d.o]));
  } else {
    if constexpr (MODE == 1) asm volatile("v_mfma_f32_16x16x32_bf16 %0, %1, %2, %0" : "+v"(s.acc[E][d.r - d.dy][d.o]) : "v"(s.B[E][d.dy * 2 + d.o]), "v"(s.A[d.r]));
    else                     asm volatile("v_mfma_f32_16x16x32_bf16 %0, %1, %2, %0" : "+v"(s.acc[E][d.r - d.dy][d.o]) : "v"(s.A[d.r]), "v"(s.B[E][d.dy * 2 + d.o]));
  }
  if constexpr (M % 3 == 0) {
    constexpr int j = M / 3;
    if constexpr (j < 6) lds_rd<(4 + j) * 1024>(s.A[4 + j], s.acur);
    else if constexpr (j == 6) lds_rd<0>(s.B[E ^ 1][0], s.bnxt);
    else if constexpr (j == 7) lds_rd<1024>(s.B[E ^ 1][1], s.bnxt);
    else if constexpr (j == 8) lds_rd<0>(s.A[0], s.anxt);
    else if constexpr (j == 9) lds_rd<4096>(s.B[E ^ 1][2], s.bnxt);
    else if constexpr (j == 10) lds_rd<4096 + 1024>(s.B[E ^ 1][3], s.bnxt);
    else if constexpr (j == 11) lds_rd<8192>(s.B[E ^ 1][4], s.bnxt);
    else if constexpr (j == 12) lds_rd<8192 + 1024>(s.B[E ^ 1][5], s.bnxt);
    else lds_rd<(j - 12) * 1024>(s.A[j - 12], s.anxt);
  }
}
template <int MODE, int E, bool FIRST, int... Ms> DI void msteps(Comp& s, std::integer_sequence<int, Ms...>) { (mstep<MODE, E, FIRST, Ms>(s), ...); }

template <int MODE, int E, bool FIRST = false> DI void unit(Comp& s, unsigned& voff, unsigned& uoff) {
  bar();
  s.acur = s.anxt;
  voff = (voff + VSLOT) == NVSLOT * VSLOT ? 0u : voff + VSLOT;
  s.anxt = s.abase + voff;
  uoff = (uoff + USLOT) == NUSLOT * USLOT ? 0u : uoff + USLOT;
  s.bnxt = s.bbase + uoff;
  msteps<MODE, E, FIRST>(s, std::make_integer_sequence<int, 48>{});
  wait_lgkm<3>();
}

struct Affine { f32x2 sa[4], sb[4]; };
DI void row_transform(const f32x4 (&ye)[2], const f32x4 (&yo)[2], const f32x4 (&hl)[2], const f32x4 (&hr)[2], const Affine& af, u32x4 (&o)[4]) {
#pragma unroll
  for (int q = 0; q < 4; ++q) {
    const f32x2 he = (q & 1) ? hi2(ye[q >> 1]) : lo2(ye[q >> 1]), ho2 = (q & 1) ? hi2(yo[q >> 1]) : lo2(yo[q >> 1]);
    const f32x2 l2 = (q & 1) ? hi2(hl[q >> 1]) : lo2(hl[q >> 1]), r2 = (q & 1) ? hi2(hr[q >> 1]) : lo2(hr[q >> 1]);
    const f32x2 d1 = silu_aff2(he, af.sa[q], af.sb[q]);
    const f32x2 d2 = silu_aff2(ho2, af.sa[q], af.sb[q]);
    const f32x2 d0 = {dpp_prev(l2[0], d2[0]), dpp_prev(l2[1], d2[1])}, d3 = {dpp_next(r2[0], d1[0]), dpp_next(r2[1], d1[1])};
    const f32x2 t0 = pk_sub(d0, d2), t1 = d1 + d2, t2 = pk_sub(d2, d1), t3 = pk_sub(d1, d3);
    o[0][q] = pack_bf16(t0[0], t0[1]); o[1][q] = pack_bf16(t1[0], t1[1]); o[2][q] = pack_bf16(t2[0], t2[1]); o[3][q] = pack_bf16(t3[0], t3[1]);
  }
}
DI void halo_ops(const char* lds, const float* c, int tx, int kg, f32x4 (&hl)[2], f32x4 (&hr)[2]) {
  const float* z = (const float*)(lds + M_ZERO);
  const float* pl = tx == 1 ? c + 8 * kg : z;
  const float* pr = tx == 0 ? c + 8 * kg : z;
  hl[0] = *(const f32x4*)pl; hl[1] = *(const f32x4*)(pl + 4);
  hr[0] = *(const f32x4*)pr; hr[1] = *(const f32x4*)(pr + 4);
}

DI f32x4 add4_here(const f32x4& a, const f32x4& b) {
  f32x2 lo, hi;
  asm volatile("v_pk_add_f32 %0, %1, %2" : "=v"(lo) : "v"(lo2(a)), "v"(lo2(b)));
  asm volatile("v_pk_add_f32 %0, %1, %2" : "=v"(hi) : "v"(hi2(a)), "v"(hi2(b)));
  return cat2(lo, hi);
}
template <int P> DI void exchange(const Comp& s, char* lds, int wid, int lane, f32x4 (&ye)[4][2], f32x4 (&yo)[4][2]) {
  char* xb = lds + wid * 16384 + lane * 16;
  const char* rb = lds + (wid ^ 1) * 16384 + lane * 16;
#pragma unroll
  for (int r = 0; r < 4; ++r)
#pragma unroll
    for (int o = 0; o < 2; ++o) {
      if constexpr (P == 0) {
        *(f32x4*)(xb + (r * 4 + o * 2) * 1024) = add4_here(s.acc[0][4 + r][o], s.acc[1][4 + r][o]);
        *(f32x4*)(xb + (r * 4 + o * 2 + 1) * 1024) = s.acc[1][4 + r][o];
      } else {
        *(f32x4*)(xb + (r * 4 + o * 2) * 1024) = s.acc[0][r][o];
        *(f32x4*)(xb + (r * 4 + o * 2 + 1) * 1024) = add4_here(s.acc[0][r][o], s.acc[1][r][o]);
      }
      __builtin_amdgcn_sched_barrier(0);
    }
  lds_bar();
#pragma unroll
  for (int o = 0; o < 2; ++o) {
    f32x4 r0[4], r1[4];
#pragma unroll
    for (int r = 0; r < 4; ++r) { r0[r] = *(const f32x4*)(rb + (r * 4 + o * 2) * 1024); r1[r] = *(const f32x4*)(rb + (r * 4 + o * 2 + 1) * 1024); }
#pragma unroll
    for (int r = 0; r < 4; ++r) {
      if constexpr (P == 0) { ye[r][o] = add4_here(s.acc[0][r][o], s.acc[1][r][o]) + r0[r]; yo[r][o] = sub4(s.acc[1][r][o], r1[r]); }
      else                  { ye[r][o] = r0[r] + s.acc[0][4 + r][o]; yo[r][o] = sub4(r1[r], add4_here(s.acc[0][4 + r][o], s.acc[1][4 + r][o])); }
    }
    __builtin_amdgcn_sched_barrier(0);
  }
}

DI void load_affine(Affine& af, const char* lds, int ho, int kg) {
  const float* red = (const float*)(lds + M_RED);
  const float mean = red[16], rstd = red[17];
  const f32x4 g0 = *(const f32x4*)(lds + M_GAM + (32 * ho + 8 * kg) * 4), g1 = *(const f32x4*)(lds + M_GAM + (32 * ho + 8 * kg) * 4 + 16);
  const f32x4 b0 = *(const f32x4*)(lds + M_BET + (32 * ho + 8 * kg) * 4), b1 = *(const f32x4*)(lds + M_BET + (32 * ho + 8 * kg) * 4 + 16);
#pragma unroll
  for (int q = 0; q < 4; ++q) {
    const f32x2 g = q == 0 ? lo2(g0) : q == 1 ? hi2(g0) : q == 2 ? lo2(g1) : hi2(g1);
    const f32x2 bb = q == 0 ? lo2(b0) : q == 1 ? hi2(b0) : q == 2 ? lo2(b1) : hi2(b1);
    af.sa[q] = g * rstd; af.sb[q] = bb - af.sa[q] * mean;
  }
}
DI void set_tag(char* ws, int bid, int word, unsigned ep, int lane) {
  if (lane == 0) __hip_atomic_store((gu32*)(ws + OFF_LINES) + bid * 32 + word, ep, RLX_AGENT);
}
template <int NQ> DI void wait_tags(char* ws, int who, int w0, bool need, unsigned ep) {
  const __amdgpu_buffer_rsrc_t rl = __builtin_amdgcn_make_buffer_rsrc((void*)(ws + OFF_LINES), (short)0, 256 * 128, 0x00020000);
  for (unsigned spins = 0;; ++spins) {
    bool ok = true;
#pragma unroll
    for (int q = 0; q < NQ; ++q) {
      const u32x4 v = __builtin_amdgcn_raw_buffer_load_b128(rl, who * 128 + w0 * 4 + q * 16, 0, 16);
      ok = ok && v[0] == ep && v[1] == ep && v[2] == ep && v[3] == ep;
    }
    if (__all(!need || ok)) break;
    if (spins > SPIN_CAP) break;
    __builtin_amdgcn_s_sleep(2);
    asm volatile("" ::: "memory");
  }
  asm volatile("" ::: "memory");
}
DI unsigned lds_key(unsigned ep, int idx) { return (ep * 16u + (unsigned)idx) * 0x9E3779B1u + 0x3C6EF372u; }
DI void lds_flag_set(char* lds, int idx, unsigned ep) {
  const unsigned key = lds_key(ep, idx);
  __hip_atomic_store((__attribute__((address_space(3))) unsigned*)(lds + M_RED + 96 + idx * 4), key, __ATOMIC_RELAXED, __HIP_MEMORY_SCOPE_WORKGROUP);
}
DI void lds_flag_wait(char* lds, int idx, unsigned ep) {
  const unsigned key = lds_key(ep, idx);
  for (unsigned spins = 0; spins < (SPIN_CAP << 4); ++spins) {
    const unsigned v = __hip_atomic_load((__attribute__((address_space(3))) unsigned*)(lds + M_RED + 96 + idx * 4), __ATOMIC_RELAXED, __HIP_MEMORY_SCOPE_WORKGROUP);
    if (__builtin_amdgcn_readfirstlane(v) == key) break;
    __builtin_amdgcn_s_sleep(1);
  }
  asm volatile("" ::: "memory");
}
DI bool lds_flag_test(char* lds, int idx, unsigned ep) {
  const unsigned v = __hip_atomic_load((__attribute__((address_space(3))) unsigned*)(lds + M_RED + 96 + idx * 4), __ATOMIC_RELAXED, __HIP_MEMORY_SCOPE_WORKGROUP);
  return __builtin_amdgcn_readfirstlane(v) == lds_key(ep, idx);
}
DI void block_stats(const float* red, float& tot, float& m2) {
  const float s0 = red[0], s1 = red[1], s2 = red[2], s3 = red[3];
  tot = (s0 + s1) + (s2 + s3);
  const float bm = tot * (1.f / 16384.f);
  const float d0 = s0 * (1.f / 4096.f) - bm, d1 = s1 * (1.f / 4096.f) - bm, d2 = s2 * (1.f / 4096.f) - bm, d3 = s3 * (1.f / 4096.f) - bm;
  m2 = (red[8] + red[9]) + (red[10] + red[11]) + 4096.f * ((d0 * d0 + d1 * d1) + (d2 * d2 + d3 * d3));
}
DI int gran_slot(const Tile& T) { return T.ty * 4 + T.tx * 2 + (T.nt & 1); }
DI void group_stats(char* ws, int conv, const Tile& T, float tot, float m2, unsigned ep, int lane) {
  gu64* gran = (gu64*)(ws + OFF_GRAN) + (conv * 8 + T.b * 4 + (T.nt >> 1)) * 64;
  if (lane < 2) {
    const float val = lane == 0 ? tot : m2;
    __hip_atomic_store(gran + gran_slot(T) * 2 + lane, ((unsigned long long)ep << 32) | (unsigned long long)__float_as_uint(val), RLX_AGENT);
  }
}
DI void group_stats_wait(char* ws, int conv, const Tile& T, float tot, float m2, unsigned ep, float* red, int lane) {
  gu64* gran = (gu64*)(ws + OFF_GRAN) + (conv * 8 + T.b * 4 + (T.nt >> 1)) * 64;
  const bool own = (lane >> 1) == gran_slot(T);
  unsigned long long g;
  for (unsigned spins = 0;; ++spins) {
    g = __hip_atomic_load(gran + lane, RLX_AGENT);
    if (__all(own || (unsigned)(g >> 32) == ep)) break;
    if (spins > SPIN_CAP) break;
    __builtin_amdgcn_s_sleep(1);
  }
  const float val = own ? ((lane & 1) ? m2 : tot) : __uint_as_float((unsigned)g);
  const float qk = dpp0<0x101>(val);
  float mean, rstd;
  gn_combine(val, qk, !(lane & 1), 16384.f, mean, rstd);
  if (lane == 0) { red[16] = mean; red[17] = rstd; }
}

DI void put_row(char* lds, __amdgpu_buffer_rsrc_t rv, const Tile& T, int ho, int rho, bool publish, int i16, int kg, const u32x4 (&o)[4]) {
  const int pos = i16 * 64 + vpos(kg, i16) * 16;
  char* d = lds + 2 * ho * VSLOT + rho * 1024 + pos;
#pragma unroll
  for (int xi = 0; xi < 4; ++xi) *(u32x4*)(d + (xi & 1) * VSLOT + (xi >> 1) * 10240) = o[xi];
  if (publish) {
    const int so = (T.b * 16 + 2 * T.nt + ho) * 524288 + (T.y0 - 1 + rho) * 2048 + T.tx * 1024;
#pragma unroll
    for (int xi = 0; xi < 4; ++xi)
      __builtin_amdgcn_raw_buffer_store_b128(o[xi], rv, pos + so + (xi & 1) * 262144 + (xi >> 1) * 131072, 0, V_STORE_AUX);
  }
}

template <int NR> struct XRows { unsigned v[NR][8]; unsigned short col[2]; u32x4 gv, bv; unsigned short gc, bc; };
DI int clamp_y(int y) { return min(max(y, 0), 63); }
template <int NR> DI void rows_stage(const int (&rho)[NR], int k0, int k1, unsigned ldsb, const u16* __restrict__ x, const Tile& T, int ho, int lane) {
  const __amdgpu_buffer_rsrc_t rx = __builtin_amdgcn_make_buffer_rsrc((void*)x, (short)0, 2 * NCH * HW * 2, 0x00020000);
  const int cl = lane >> 3;
#pragma unroll
  for (int k = 0; k < NR; ++k)
#pragma unroll
    for (int g = 0; g < 4; ++g) {
      if (k < k0 || k >= k1) continue;
      const unsigned vo = (unsigned)(cl * (HW * 2) + (((lane & 7) - 4 * (g & 1)) & 7) * 16);
      const unsigned so = (unsigned)(((T.b * NCH + T.nt * 64 + 32 * ho + 8 * g) * HW + clamp_y(T.y0 - 1 + rho[k]) * 64) * 2);
      dma1(ldsb + (rho[k] * 2 + ho) * 4096 + g * 1024, vo, rx, so);
    }
}
template <int NR> DI void rows_params(XRows<NR>& X, const u16* __restrict__ gam, const u16* __restrict__ bet, const Tile& T, int ho, int lane) {
  const int kg = lane >> 4, chl = lane & 31, ch = T.nt * 64 + 32 * ho;
  X.gv = *(const u32x4*)(gam + ch + 8 * kg); X.bv = *(const u32x4*)(bet + ch + 8 * kg);
  X.gc = gam[ch + chl]; X.bc = bet[ch + chl];
}
template <int NR> DI void rows_fetch(XRows<NR>& X, const int (&rho)[NR], int k0, int k1, const char* lds, const Tile& T, int ho, int lane) {
  const int i16 = lane & 15, kg = lane >> 4;
  const int pos = (((T.tx * 4 + (i16 >> 2)) + 4 * (kg & 1)) & 7) * 16 + (i16 & 3) * 4;
#pragma unroll
  for (int k = 0; k < NR; ++k)
#pragma unroll
    for (int j = 0; j < 8; ++j)
      if (k >= k0 && k < k1) X.v[k][j] = *(const unsigned*)(lds + (rho[k] * 2 + ho) * 4096 + (8 * kg + j) * 128 + pos);
}
template <int NR> DI void rows_fetch_col(XRows<NR>& X, int rc0, int rc1, const char* lds, const Tile& T, int ho, int lane) {
  const int chl = lane & 31;
  const int xc = T.tx == 0 ? 32 : 31;
  const int cpos = chl * 128 + (((xc >> 3) + 4 * ((chl >> 3) & 1)) & 7) * 16 + (xc & 7) * 2;
  X.col[0] = *(const unsigned short*)(lds + (rc0 * 2 + ho) * 4096 + cpos);
  X.col[1] = *(const unsigned short*)(lds + (rc1 * 2 + ho) * 4096 + cpos);
}
template <int NR, int NI> DI void rows_stats(const XRows<NR>& X, float* red, int wid, int lane) {
  const float piv = __builtin_bit_cast(float, __builtin_amdgcn_readfirstlane(X.v[0][0] << 16));
  f32x2 sv = {0.f, 0.f}, qv = {0.f, 0.f};
#pragma unroll
  for (int k = 0; k < NI; ++k)
#pragma unroll
    for (int j = 0; j < 8; ++j) {
      const f32x2 d = f32x2{bf_lo(X.v[k][j]), bf_hi(X.v[k][j])} - piv;
      sv += d; qv = __builtin_elementwise_fma(d, d, qv);
    }
  const float s = wave_sum(sv[0] + sv[1]), q = wave_sum(qv[0] + qv[1]);
  if (lane == 0) { red[wid] = s + piv * (float)(NI * 1024); red[8 + wid] = q - s * s * (1.f / (float)(NI * 1024)); }
}
template <int NR> DI void rows_emit(const XRows<NR>& X, const int (&rho)[NR], int k0, int k1, char* lds, char* ws, const Tile& T, int wid, int ho, int lane) {
  const int i16 = lane & 15, kg = lane >> 4;
  const float* red = (const float*)(lds + M_RED);
  const float mean = red[16], rstd = red[17];
  float sa[8], sb[8];
#pragma unroll
  for (int j = 0; j < 8; ++j) {
    sa[j] = rstd * ((j & 1) ? bf_hi(X.gv[j >> 1]) : bf_lo(X.gv[j >> 1]));
    sb[j] = ((j & 1) ? bf_hi(X.bv[j >> 1]) : bf_lo(X.bv[j >> 1])) - mean * sa[j];
  }
  const float* col = (const float*)(lds + M_COL + wid * 512);
  const __amdgpu_buffer_rsrc_t rv = __builtin_amdgcn_make_buffer_rsrc((void*)(ws + OFF_V1), (short)0, (int)V_BYTES, 0x00020000);
#pragma unroll
  for (int k = 0; k < NR; ++k) {
    if (k < k0 || k >= k1) continue;
    f32x4 hl[2], hr[2];
    halo_ops(lds, col + k * 32, T.tx, kg, hl, hr);
    float t[4][8];
#pragma unroll
    for (int j = 0; j < 8; ++j) {
      const f32x2 a2 = {sa[j], sa[j]}, b2 = {sb[j], sb[j]};
      const f32x2 d = silu_aff2(f32x2{bf_lo(X.v[k][j]), bf_hi(X.v[k][j])}, a2, b2);
      t[0][j] = dpp_prev(hl[j >> 2][j & 3], d[1]) - d[1];
      t[1][j] = d[0] + d[1];
      t[2][j] = d[1] - d[0];
      t[3][j] = d[0] - dpp_next(hr[j >> 2][j & 3], d[0]);
    }
    u32x4 o[4];
#pragma unroll
    for (int xi = 0; xi < 4; ++xi)
      o[xi] = u32x4{pack_bf16(t[xi][0], t[xi][1]), pack_bf16(t[xi][2], t[xi][3]), pack_bf16(t[xi][4], t[xi][5]), pack_bf16(t[xi][6], t[xi][7])};
    const int y = T.y0 - 1 + rho[k];
    const bool halo_row = NR == 1;
    if (halo_row && (y < 0 || y > 63)) { o[0] = o[1] = o[2] = o[3] = u32x4{0u, 0u, 0u, 0u}; }
    put_row(lds, rv, T, ho, rho[k], !halo_row, i16, kg, o);
    __builtin_amdgcn_sched_barrier(0);
  }
}
template <int NR> DI void rows_column(const XRows<NR>& X, char* lds, int wid, int lane) {
  const float* red = (const float*)(lds + M_RED);
  const float mean = red[16], rstd = red[17];
  float* col = (float*)(lds + M_COL + wid * 512);
  const float sa = rstd * __uint_as_float((unsigned)X.gc << 16), sb = __uint_as_float((unsigned)X.bc << 16) - mean * sa;
  col[lane] = silu_f(__uint_as_float((unsigned)X.col[0] << 16) * sa + sb);
  if constexpr (NR > 2) col[64 + lane] = silu_f(__uint_as_float((unsigned)X.col[1] << 16) * sa + sb);
}

DI int u_row(int conv, int of, int i) { return conv == 0 ? (32 * (of >> 1) + 8 * (i >> 2) + 4 * (of & 1) + (i & 3)) : (16 * of + i); }
DI void u_load(u32x4 (&d)[9], const u16* __restrict__ w, int conv, const Tile& T, int uidx, int of, int lane) {
  const int i = lane & 15, kg = lane >> 4;
  const u16* src = w + ((size_t)(T.nt * 64 + u_row(conv, of, i)) * 512 + 32 * (uidx >> 1) + 8 * kg) * 9;
  asm volatile("global_load_dwordx4 %0, %9, off\n\tglobal_load_dwordx4 %1, %9, off offset:16\n\tglobal_load_dwordx4 %2, %9, off offset:32\n\t"
               "global_load_dwordx4 %3, %9, off offset:48\n\tglobal_load_dwordx4 %4, %9, off offset:64\n\tglobal_load_dwordx4 %5, %9, off offset:80\n\t"
               "global_load_dwordx4 %6, %9, off offset:96\n\tglobal_load_dwordx4 %7, %9, off offset:112\n\tglobal_load_dwordx4 %8, %9, off offset:128"
               : "=&v"(d[0]), "=&v"(d[1]), "=&v"(d[2]), "=&v"(d[3]), "=&v"(d[4]), "=&v"(d[5]), "=&v"(d[6]), "=&v"(d[7]), "=&v"(d[8])
               : "v"(src) : "memory");
}
DI void u_landed(u32x4 (&d)[9]) {
  asm volatile("" : "+v"(d[0]), "+v"(d[1]), "+v"(d[2]), "+v"(d[3]), "+v"(d[4]), "+v"(d[5]), "+v"(d[6]), "+v"(d[7]), "+v"(d[8]) :: "memory");
}
DI void u_stage(unsigned ldsdst, const u16* __restrict__ w, int conv, const Tile& T, int uidx, int of, int lane) {
  const __amdgpu_buffer_rsrc_t rw = __builtin_amdgcn_make_buffer_rsrc((void*)w, (short)0, 512 * 512 * 9 * 2, 0x00020000);
#pragma unroll
  for (int k = 0; k < 9; ++k) {
    const int t = k * 64 + lane, row = t / 36, c16 = t - row * 36;
    dma1(ldsdst + k * 1024, (unsigned)(((T.nt * 64 + u_row(conv, of, row)) * 512 + 32 * (uidx >> 1)) * 18 + c16 * 16), rw, 0u);
  }
}
DI void u_touch(unsigned ldsb, const u16* __restrict__ w, int conv, const Tile& T, int uidx, int of, int lane) {
  const __amdgpu_buffer_rsrc_t rw = __builtin_amdgcn_make_buffer_rsrc((void*)w, (short)0, 512 * 512 * 9 * 2, 0x00020000);
#pragma unroll
  for (int h = 0; h < 2; ++h) {
    const int t = min(64 * h + lane, 79), row = t / 5;
    const unsigned vo = (unsigned)(((T.nt * 64 + u_row(conv, of, row)) * 512 + 32 * (uidx >> 1)) * 18 + (t - row * 5) * 128);
    asm volatile("s_mov_b32 m0, %0\n\ts_nop 0\n\tbuffer_load_dword %1, %2, 0 offen lds" :: "s"(ldsb + M_DUMP), "v"(vo), "s"(rw) : "memory");
  }
}
DI void u_fetch(u32x4 (&d)[9], const char* stage, int lane) {
#pragma unroll
  for (int k = 0; k < 9; ++k) d[k] = *(const u32x4*)(stage + (lane & 15) * 576 + (lane >> 4) * 144 + k * 16);
}
DI void u_emit(const u32x4 (&d)[9], char* ws, int conv, const Tile& T, int uidx, int of, int lane) {
  const __amdgpu_buffer_rsrc_t ru = __builtin_amdgcn_make_buffer_rsrc((void*)(ws + (conv == 0 ? OFF_U1 : OFF_U2)), (short)0, (int)U_BYTES, 0x00020000);
  const int dst = T.nt * 786432 + uidx * USLOT + of * 1024 + lane * 16;
  const float sg = (uidx & 1) ? 1.f : -1.f;
#pragma unroll
  for (int dy = 0; dy < 3; ++dy) {
    float ua[8], ub[8];
#pragma unroll
    for (int j = 0; j < 8; ++j) {
      float g[3];
#pragma unroll
      for (int dx = 0; dx < 3; ++dx) {
        const int e = 9 * j + 3 * dy + dx;
        const unsigned dw = d[e >> 3][(e >> 1) & 3];
        g[dx] = (e & 1) ? bf_hi(dw) : bf_lo(dw);
      }
      const float mid = 0.5f * (g[0] + sg * g[1] + g[2]);
      ua[j] = (uidx & 1) ? mid : g[0];
      ub[j] = (uidx & 1) ? g[2] : mid;
    }
    const u32x4 oa = {pack_bf16(ua[0], ua[1]), pack_bf16(ua[2], ua[3]), pack_bf16(ua[4], ua[5]), pack_bf16(ua[6], ua[7])};
    const u32x4 ob = {pack_bf16(ub[0], ub[1]), pack_bf16(ub[2], ub[3]), pack_bf16(ub[4], ub[5]), pack_bf16(ub[6], ub[7])};
    __builtin_amdgcn_raw_buffer_store_b128(oa, ru, dst + dy * 4096, 0, V_STORE_AUX);
    __builtin_amdgcn_raw_buffer_store_b128(ob, ru, dst + 12288 + dy * 4096, 0, V_STORE_AUX);
  }
}

template <int P> DI void epi1_mfma(const Comp& s, char* lds, char* ws, const Tile& T, unsigned ep, int wid, int lane) {
  constexpr int MODE = 1; (void)MODE;
  const int i16 = lane & 15, kg = lane >> 4, ho = wid >> 1;
  f32x4 ye[4][2], yo[4][2];
  exchange<P>(s, lds, wid, lane, ye, yo);
  if (wid == 0) { STAMP(12); }
  {
    const f32x4 e0 = *(const f32x4*)(lds + M_EMB + (32 * ho + 8 * kg) * 4), e1 = *(const f32x4*)(lds + M_EMB + (32 * ho + 8 * kg) * 4 + 16);
#pragma unroll
    for (int r = 0; r < 4; ++r) { ye[r][0] += e0; ye[r][1] += e1; yo[r][0] += e0; yo[r][1] += e1; }
  }
  const __amdgpu_buffer_rsrc_t rh = __builtin_amdgcn_make_buffer_rsrc((void*)(ws + OFF_HALO), (short)0, 256 * HALO_STRIDE, 0x00020000);
  float* red = (float*)(lds + M_RED);
  const int hb = T.bid * HALO_STRIDE;
  constexpr int RE = P == 0 ? 0 : 3;
#pragma unroll
  for (int f = 0; f < 2; ++f) {
    __builtin_amdgcn_raw_buffer_store_b128(__builtin_bit_cast(u32x4, ye[RE][f]), rh, hb + P * 8192 + ((ho * 2 + f) * 2) * 1024 + lane * 16, 0, 16);
    __builtin_amdgcn_raw_buffer_store_b128(__builtin_bit_cast(u32x4, yo[RE][f]), rh, hb + P * 8192 + ((ho * 2 + f) * 2 + 1) * 1024 + lane * 16, 0, 16);
  }
  if (i16 == (T.tx == 0 ? 15 : 0)) {
#pragma unroll
    for (int r = 0; r < 4; ++r)
#pragma unroll
      for (int f = 0; f < 2; ++f)
        __builtin_amdgcn_raw_buffer_store_b128(__builtin_bit_cast(u32x4, T.tx == 0 ? yo[r][f] : ye[r][f]), rh,
                                               hb + 16384 + (4 * P + r) * 256 + (32 * ho + 8 * kg + 4 * f) * 4, 0, 16);
  }
  {
    const float piv = __builtin_bit_cast(float, __builtin_amdgcn_readfirstlane(__builtin_bit_cast(int, ye[0][0][0])));
    f32x4 sv = {0.f, 0.f, 0.f, 0.f}, qv = {0.f, 0.f, 0.f, 0.f};
#pragma unroll
    for (int r = 0; r < 4; ++r)
#pragma unroll
      for (int f = 0; f < 2; ++f) {
        const f32x4 d0 = ye[r][f] - piv, d1 = yo[r][f] - piv;
        sv += d0 + d1;
        qv = __builtin_elementwise_fma(d0, d0, qv);
        qv = __builtin_elementwise_fma(d1, d1, qv);
      }
    const float sum = wave_sum((sv[0] + sv[1]) + (sv[2] + sv[3])), q = wave_sum((qv[0] + qv[1]) + (qv[2] + qv[3]));
    if (lane == 0) { red[wid] = sum + piv * 4096.f; red[8 + wid] = q - sum * sum * (1.f / 4096.f); }
  }
  lds_bar();
  if (wid == 0) { STAMP(13); }
  wait_vm<0>();
  set_tag(ws, T.bid, W_HALO + wid, ep, lane);
  const int chl = lane & 31, half = lane >> 5;
  const float gam_c = ((const float*)(lds + M_GAM))[32 * ho + chl], bet_c = ((const float*)(lds + M_BET))[32 * ho + chl];
  lds_bar();
  if (wid == 0) { STAMP(11); }
  Affine af;
  load_affine(af, lds, ho, kg);
  float* col = (float*)(lds + M_COL + wid * 512);
  {
    const float* raw = (const float*)(lds + M_RAW);
    const float sa = red[17] * gam_c, sb = bet_c - red[16] * sa;
    col[lane] = silu_f(raw[(4 * P + 1 + half) * 64 + 32 * ho + chl] * sa + sb);
    col[64 + lane] = silu_f(raw[(4 * P + 3 + half) * 64 + 32 * ho + chl] * sa + sb);
  }
  if (wid == 0) { STAMP(18); }
  const __amdgpu_buffer_rsrc_t rv = __builtin_amdgcn_make_buffer_rsrc((void*)(ws + OFF_V2), (short)0, (int)V_BYTES, 0x00020000);
#pragma unroll
  for (int k = 0; k < 4; ++k) {
    f32x4 hl[2], hr[2];
    halo_ops(lds, col + k * 32, T.tx, kg, hl, hr);
    u32x4 o[4];
    row_transform(ye[k], yo[k], hl, hr, af, o);
    put_row(lds, rv, T, ho, 4 * P + k + 1, true, i16, kg, o);
    __builtin_amdgcn_sched_barrier(0);
  }
  if (wid == 0) { STAMP(19); }
}

DI void epi1_loader(const Loader& L, const u16* __restrict__ w2, int uprod, int uidx, char* lds, char* ws, const Tile& T, unsigned ep, int wid, int lane) {
  constexpr int MODE = 1; (void)MODE;
  const int i16 = lane & 15, kg = lane >> 4, p = wid & 1, ho = (wid >> 1) & 1;
  const __amdgpu_buffer_rsrc_t rh = __builtin_amdgcn_make_buffer_rsrc((void*)(ws + OFF_HALO), (short)0, 256 * HALO_STRIDE, 0x00020000);
  float* red = (float*)(lds + M_RED);
  {
    u32x4 dw2[9];
    u_load(dw2, w2, 1, T, uidx, wid - 4, lane);
    lds_bar();
    bar();
    wait_vm<0>();
    u_landed(dw2);
    u_emit(dw2, ws, 1, T, uidx, wid - 4, lane);
  }
  bar();
  float tot = 0.f, m2 = 0.f;
  if (wid == 4) {
    block_stats(red, tot, m2);
    group_stats(ws, 1, T, tot, m2, ep, lane);
    STAMP(14); STAMPR(16);
  }
  wait_vm<0>();
  set_tag(ws, T.bid, W_U2 + (wid - 4), ep, lane);
  if (wid == 4) {
    group_stats_wait(ws, 1, T, tot, m2, ep, red, lane);
    STAMP(15); STAMPR(17);
  }
  const int tyn = p == 0 ? T.ty - 1 : T.ty + 1;
  const bool have_v = tyn >= 0 && tyn < 8;
  const int bid_v = tile_bid(T.b, have_v ? tyn : T.ty, T.tx, T.nt);
  f32x4 ve[2], vo[2];
  const int hbv = bid_v * HALO_STRIDE + (p == 0 ? 8192 : 0) + ho * 4096 + lane * 16;
  if (wid != 4) {
    wait_tags<1>(ws, bid_v, W_HALO, have_v, ep);
#pragma unroll
    for (int f = 0; f < 2; ++f) {
      ve[f] = __builtin_bit_cast(f32x4, __builtin_amdgcn_raw_buffer_load_b128(rh, hbv + f * 2048, 0, 16));
      vo[f] = __builtin_bit_cast(f32x4, __builtin_amdgcn_raw_buffer_load_b128(rh, hbv + f * 2048 + 1024, 0, 16));
    }
  }
  if (wid == 5) {
    const int nb = T.bid ^ 32;
    wait_tags<1>(ws, nb, W_HALO, true, ep);
    const u32x4 c0 = __builtin_amdgcn_raw_buffer_load_b128(rh, nb * HALO_STRIDE + 16384 + lane * 16, 0, 16);
    const u32x4 c1 = __builtin_amdgcn_raw_buffer_load_b128(rh, nb * HALO_STRIDE + 16384 + 1024 + lane * 16, 0, 16);
    *(u32x4*)(lds + M_RAW + 256 + lane * 16) = c0;
    *(u32x4*)(lds + M_RAW + 256 + 1024 + lane * 16) = c1;
  } else if (wid >= 6) {
    const int tyd = wid == 6 ? T.ty - 1 : T.ty + 1;
    float cv = 0.f;
    if (tyd >= 0 && tyd < 8) {
      const int nb = tile_bid(T.b, tyd, T.tx ^ 1, T.nt);
      wait_tags<1>(ws, nb, W_HALO, true, ep);
      cv = __uint_as_float(__builtin_amdgcn_raw_buffer_load_b32(rh, nb * HALO_STRIDE + 16384 + (wid == 6 ? 7 : 0) * 256 + lane * 4, 0, 16));
    }
    ((float*)(lds + M_RAW))[(wid == 6 ? 0 : 9) * 64 + lane] = cv;
  }
  const int chl = lane & 31;
  const float gam_c = ((const float*)(lds + M_GAM))[32 * ho + chl], bet_c = ((const float*)(lds + M_BET))[32 * ho + chl];
  lds_bar();
  if (wid == 4) {
    wait_tags<1>(ws, bid_v, W_HALO, have_v, ep);
#pragma unroll
    for (int f = 0; f < 2; ++f) {
      ve[f] = __builtin_bit_cast(f32x4, __builtin_amdgcn_raw_buffer_load_b128(rh, hbv + f * 2048, 0, 16));
      vo[f] = __builtin_bit_cast(f32x4, __builtin_amdgcn_raw_buffer_load_b128(rh, hbv + f * 2048 + 1024, 0, 16));
    }
  }
  Affine af;
  load_affine(af, lds, ho, kg);
  float* col = (float*)(lds + M_COL + wid * 512);
  {
    const float* raw = (const float*)(lds + M_RAW);
    const float sa = red[17] * gam_c, sb = bet_c - red[16] * sa;
    col[lane] = silu_f(raw[9 * p * 64 + 32 * ho + chl] * sa + sb);
  }
  if (wid == 5) {
    wait_tags<1>(ws, uprod, W_U2, lane < 32 && lane != uidx, ep);
    lds_flag_set(lds, 3, ep);
  } else {
    lds_flag_wait(lds, 3, ep);
  }
  asm volatile("" : "+v"(ve[0]), "+v"(ve[1]), "+v"(vo[0]), "+v"(vo[1]));
  ld_u(L, 0); ld_u(L, 1); ld_u(L, 2);
  const __amdgpu_buffer_rsrc_t rv = __builtin_amdgcn_make_buffer_rsrc((void*)(ws + OFF_V2), (short)0, (int)V_BYTES, 0x00020000);
  f32x4 hl[2], hr[2];
  u32x4 o[4];
  halo_ops(lds, col, T.tx, kg, hl, hr);
  row_transform(ve, vo, hl, hr, af, o);
  if (!have_v) { o[0] = o[1] = o[2] = o[3] = u32x4{0u, 0u, 0u, 0u}; }
  put_row(lds, rv, T, ho, 9 * p, false, i16, kg, o);
  wait_vm<0>();
}

constexpr int xpiece(int k) { return k < 24 ? LDS_U + 2 * USLOT + k * 1024 : LDS_U + (k - 24) * 1024; }

template <int P> DI void epi2_mfma(const Comp& s, char* lds, char* ws, const Tile& T, u16* __restrict__ out, int wid, int lane) {
  constexpr int MODE = 2; (void)MODE; (void)ws;
  const int i16 = lane & 15, kg = lane >> 4, ho = wid >> 1;
  bar();
  f32x4 ye[4][2], yo[4][2];
  exchange<P>(s, lds, wid, lane, ye, yo);
  if (wid == 0) { STAMP(12); }
  const size_t off0 = ((size_t)(T.b * NCH + T.nt * 64 + 32 * ho + i16)) * HW + (T.y0 + 4 * P) * 64 + 32 * T.tx + 8 * kg;
  [[maybe_unused]] const __amdgpu_buffer_rsrc_t ro = __builtin_amdgcn_make_buffer_rsrc((void*)out, (short)0, 2 * NCH * HW * 2, 0x00020000);
#pragma unroll
  for (int f = 0; f < 2; ++f)
#pragma unroll
    for (int r = 0; r < 4; ++r) {
      const int k = (ho * 2 + f) * 8 + 4 * P + r;
      const u32x4 xv = *(const u32x4*)(lds + (k < 24 ? LDS_U + 2 * USLOT + k * 1024 : LDS_U + (k - 24) * 1024) + lane * 16);
      u32x4 o;
#pragma unroll
      for (int j = 0; j < 4; ++j) o[j] = pack_bf16(ye[r][f][j] + bf_lo(xv[j]), yo[r][f][j] + bf_hi(xv[j]));
      __builtin_amdgcn_raw_buffer_store_b128(o, ro, (int)((off0 + (size_t)f * 16 * HW + r * 64) * 2), 0, OUT_AUX);
    }
}

template <int MODE> DI void loader_loop(const Loader& L, int uidx, int uprod, u32x4 (&ep8)[8], char* lds, char* ws, const Tile& T, unsigned ep, int w, int lane) {
  const int pty = T.ty - 1 + (lane >> 3);
  const bool need = lane < 24 && pty >= 0 && pty < 8 && (lane & 7) != T.nt;
  const int pbid = need ? tile_bid(T.b, pty, T.tx, lane & 7) : T.bid;
  constexpr int WV = MODE == 1 ? W_V1 : W_V2;
  lds_bar();
  if (w == 0) { STAMPR(20); }
  u32x4 tg = {0u, 0u, 0u, 0u};
  if (w == 0) {
    const char* tp = ws + OFF_LINES + pbid * 128 + WV * 4;
    asm volatile("global_load_dwordx4 %0, %1, off sc1" : "=&v"(tg) : "v"(tp) : "memory");
  }
  bar();
  ld_u(L, 3);
  if constexpr (MODE == 1) {
    if (w == 1) { if (!lds_flag_test(lds, 12, ep)) { wait_tags<1>(ws, uprod, W_U1, lane < 32 && lane != uidx, ep); lds_flag_set(lds, 12, ep); } }
    else lds_flag_wait(lds, 12, ep);
  }
  if (MODE == 1 && w == 3) {
    const __amdgpu_buffer_rsrc_t re = __builtin_amdgcn_make_buffer_rsrc((void*)(ws + OFF_EMBP), (short)0, 8 * 32 * 512, 0x00020000);
#pragma unroll
    for (int i = 0; i < 8; ++i) ep8[i] = __builtin_amdgcn_raw_buffer_load_b128(re, (T.nt * 32 + (lane >> 4) * 8 + i) * 512 + T.b * 256 + (lane & 15) * 16, 0, 16);
  }
  bar();
  ld_u(L, 4);
  bool early = false;
  if (w == 0) {
    wait_vm<12>();
    asm volatile("" : "+v"(tg) :: "memory");
    early = __all(!need || (tg[0] == ep && tg[1] == ep && tg[2] == ep && tg[3] == ep));
    if (early) lds_flag_set(lds, MODE, ep);
  }
  wait_vm<6>();
  bar();
  if (w == 0) {
    STAMPR(22);
    if (!early) { wait_tags<1>(ws, pbid, WV, need, ep); lds_flag_set(lds, MODE, ep); }
    STAMPR(23);
  } else {
    lds_flag_wait(lds, MODE, ep);
  }
  ld_v(L, 4); ld_v(L, 5); ld_u(L, 5);
  wait_vm<11>();
  bar();
#pragma unroll 1
  for (int t = 4; t < NUNIT - 2; ++t) {
    ld_v(L, t + 2); ld_u(L, t + 2);
    wait_vm<11>();
    bar();
  }
}

template <int MODE> DI void mfma_loop(Comp& s, char* ws, const u16* __restrict__ w2, const Tile& T, unsigned ldsb, unsigned ep, int wid, int lane) {
  const int bid = T.bid;
  lds_bar();
  lds_rd<0>(s.B[0][0], s.bbase); lds_rd<1024>(s.B[0][1], s.bbase); lds_rd<0>(s.A[0], s.abase);
  lds_rd<4096>(s.B[0][2], s.bbase); lds_rd<5120>(s.B[0][3], s.bbase); lds_rd<8192>(s.B[0][4], s.bbase); lds_rd<9216>(s.B[0][5], s.bbase);
  lds_rd<1024>(s.A[1], s.abase); lds_rd<2048>(s.A[2], s.abase); lds_rd<3072>(s.A[3], s.abase);
  wait_lgkm<3>();
  s.anxt = s.abase;
  unsigned voff = 0, uoff = 0;
  unit<MODE, 0, true>(s, voff, uoff);
  wait_vm<0>();
  set_tag(ws, bid, (MODE == 1 ? W_V1 : W_V2) + wid, ep, fresh_lane());
  if (MODE == 1) u_touch(ldsb, w2, 1, T, (T.b * 8 + T.ty) * 2 + T.tx, wid, fresh_lane());
  unit<MODE, 1, true>(s, voff, uoff);
#pragma unroll 1
  for (int c = 1; c < 16; ++c) {
    unit<MODE, 0>(s, voff, uoff);
    unit<MODE, 1>(s, voff, uoff);
  }
  asm volatile("s_waitcnt lgkmcnt(0)\n\ts_nop 15" ::: "memory");
}
__global__ __launch_bounds__(512, 2) void k_main(const u16* __restrict__ x, const u16* __restrict__ temb, const u16* __restrict__ g1, const u16* __restrict__ be1,
                                                 const u16* __restrict__ w1, const u16* __restrict__ wemb, const u16* __restrict__ g2,
                                                 const u16* __restrict__ be2, const u16* __restrict__ w2, u16* __restrict__ out, char* __restrict__ ws) {
  __shared__ __attribute__((aligned(16))) char lds[LDS_TOTAL];
  const int tid = threadIdx.x, lane = tid & 63;
  const int wid = __builtin_amdgcn_readfirstlane(tid >> 6);
  const int bid = blockIdx.x, xcd = bid & 7, slot = bid >> 3;
  Tile T;
  T.bid = bid; T.b = xcd >> 2; T.nt = (xcd & 1) * 4 + (slot & 3); T.ty = ((xcd >> 1) & 1) * 4 + (slot >> 3); T.tx = (slot >> 2) & 1; T.y0 = T.ty * 8;
  const unsigned ldsb = (unsigned)(size_t)lds;
  const int p = wid & 1, ho = (wid >> 1) & 1;
  {
    const __amdgpu_buffer_rsrc_t rc = __builtin_amdgcn_make_buffer_rsrc((void*)__builtin_amdgcn_s_getpc(), (short)0, CODE_BYTES, 0x00020000);
    asm volatile("s_mov_b32 m0, %0\n\ts_nop 0\n\tbuffer_load_dword %1, %2, 0 offen lds" :: "s"(ldsb + M_DUMP), "v"((unsigned)tid * 128u), "s"(rc) : "memory");
  }
  const unsigned ep = __builtin_amdgcn_readfirstlane(__hip_atomic_load((gu32*)(ws + OFF_EPOCH), RLX_AGENT)) + 1u;
  float* red = (float*)(lds + M_RED);

  if (wid >= 4) {
    const int w = wid - 4;
    const int uidx = (T.b * 8 + T.ty) * 2 + T.tx;
    const int rho[1] = {9 * p};
    const char* stage = lds + LDS_U + w * 9216;
    XRows<1> X;
    rows_load<1>(X, rho, ldsb, x, g1, be1, T, ho, lane);
    u32x4 we[3];
    unsigned short te[2][3];
    if (w == 3) {
#pragma unroll
      for (int i = 0; i < 3; ++i) {
        const int k = 24 * uidx + 3 * (lane >> 3) + i;
        we[i] = *(const u32x4*)(wemb + (size_t)k * 512 + T.nt * 64 + 8 * (lane & 7));
        te[0][i] = temb[k]; te[1][i] = temb[NEMB + k];
      }
    }
    wait_vm<0>();
    u_stage(ldsb + LDS_U + w * 9216, w1, 0, T, uidx, w, lane);
    rows_fetch<1>(X, rho, 9 * p, 9 * p, lds, T, ho, lane);
    wait_vm<0>();
    { constexpr int MODE = 1; (void)MODE; if (w == 1) { STAMPR(48); } }
    {
      u32x4 dw1[9];
      u_fetch(dw1, stage, lane);
      u_emit(dw1, ws, 0, T, uidx, w, lane);
    }
    { constexpr int MODE = 1; (void)MODE; if (w == 1) { STAMPR(50); } }
    if (w == 3) {
      float a[2][8];
#pragma unroll
      for (int j = 0; j < 8; ++j) a[0][j] = a[1][j] = 0.f;
#pragma unroll
      for (int i = 0; i < 3; ++i) {
        const float t0 = silu_f(__uint_as_float((unsigned)te[0][i] << 16)), t1 = silu_f(__uint_as_float((unsigned)te[1][i] << 16));
#pragma unroll
        for (int j = 0; j < 8; ++j) {
          const float wv = (j & 1) ? bf_hi(we[i][j >> 1]) : bf_lo(we[i][j >> 1]);
          a[0][j] += t0 * wv; a[1][j] += t1 * wv;
        }
      }
      float* sc = (float*)(lds + M_COL);
#pragma unroll
      for (int j = 0; j < 8; ++j) { sc[(lane >> 3) * 128 + (lane & 7) * 8 + j] = a[0][j]; sc[(lane >> 3) * 128 + 64 + (lane & 7) * 8 + j] = a[1][j]; }
      float e0 = 0.f, e1 = 0.f;
#pragma unroll
      for (int g = 0; g < 8; ++g) { e0 += sc[g * 128 + lane]; e1 += sc[g * 128 + 64 + lane]; }
      const __amdgpu_buffer_rsrc_t re = __builtin_amdgcn_make_buffer_rsrc((void*)(ws + OFF_EMBP), (short)0, 8 * 32 * 512, 0x00020000);
      __builtin_amdgcn_raw_buffer_store_b32(__float_as_uint(e0), re, (T.nt * 32 + uidx) * 512 + lane * 4, 0, 16);
      __builtin_amdgcn_raw_buffer_store_b32(__float_as_uint(e1), re, (T.nt * 32 + uidx) * 512 + 256 + lane * 4, 0, 16);
    }
    wait_vm<0>();
    set_tag(ws, T.bid, W_U1 + w, ep, lane);
    { constexpr int MODE = 1; (void)MODE; if (w == 1) { STAMPR(52); } }
    lds_bar();
    Loader L;
    L.nt = T.nt;
    L.rv = __builtin_amdgcn_make_buffer_rsrc((void*)(ws + OFF_V1), (short)0, (int)V_BYTES, 0x00020000);
    L.ru = __builtin_amdgcn_make_buffer_rsrc((void*)(ws + OFF_U1), (short)0, (int)U_BYTES, 0x00020000);
    L.vmid = lane * 16;
    L.vfirst = (T.y0 == 0 && !(w & 1)) ? 0x80000000u : L.vmid;
    L.vlast = (T.y0 == 56 && (w & 1)) ? 0x80000000u : L.vmid;
    L.sv = (unsigned)T.b * 16u * 524288u + (unsigned)(w >> 1) * 131072u + (unsigned)((T.y0 + 5 * (w & 1)) * 2048 + T.tx * 1024) - 2048u;
    L.su = (unsigned)T.nt * 786432u + (unsigned)w * 6144u;
    L.mv = ldsb + (w >> 1) * 10240 + (w & 1) * 5120;
    L.mu = ldsb + LDS_U + w * 6144;
    const int uprod = tile_bid((lane >> 4) & 1, (lane >> 1) & 7, lane & 1, T.nt);
    u32x4 tg = {0u, 0u, 0u, 0u};
    if (w == 1) {
      const char* tp = ws + OFF_LINES + uprod * 128 + W_U1 * 4;
      asm volatile("global_load_dwordx4 %0, %1, off sc1" : "=&v"(tg) : "v"(tp) : "memory");
    }
    rows_column<1>(X, lds, wid, lane);
    rows_emit<1>(X, rho, 0, 1, lds, ws, T, wid, ho, lane);
    if (w == 1) {
      wait_vm<0>();
      asm volatile("" : "+v"(tg) :: "memory");
      const bool ok = tg[0] == ep && tg[1] == ep && tg[2] == ep && tg[3] == ep;
      const bool need = lane < 32 && lane != uidx, own = need && (lane >> 2) == T.nt;
      if (!__all(!own || ok)) wait_tags<1>(ws, uprod, W_U1, own, ep);
      if (__all(!need || ok)) lds_flag_set(lds, 12, ep);
      lds_flag_set(lds, 0, ep);
    } else {
      lds_flag_wait(lds, 0, ep);
    }
    { constexpr int MODE = 1; (void)MODE; if (w == 1) { STAMPR(53); } }
    ld_u(L, 0); ld_u(L, 1); ld_u(L, 2);
    wait_vm<0>();
    u32x4 ep8[8];
    loader_loop<1>(L, uidx, uprod, ep8, lds, ws, T, ep, w, lane);
    if (w == 3) {
      f32x4 e = {0.f, 0.f, 0.f, 0.f};
#pragma unroll
      for (int i = 0; i < 8; ++i) e += __builtin_bit_cast(f32x4, ep8[i]);
#pragma unroll
      for (int j = 0; j < 4; ++j) { e[j] += __shfl_xor(e[j], 16); e[j] += __shfl_xor(e[j], 32); }
      if (lane < 16) *(f32x4*)(lds + M_EMB + lane * 16) = e;
    }
    wait_vm<0>();
    bar();
    bar();
    L.rv = __builtin_amdgcn_make_buffer_rsrc((void*)(ws + OFF_V2), (short)0, (int)V_BYTES, 0x00020000);
    L.ru = __builtin_amdgcn_make_buffer_rsrc((void*)(ws + OFF_U2), (short)0, (int)U_BYTES, 0x00020000);
    epi1_loader(L, w2, uprod, uidx, lds, ws, T, ep, wid, lane);
    loader_loop<2>(L, uidx, uprod, ep8, lds, ws, T, ep, w, lane);
    {
      const __amdgpu_buffer_rsrc_t rx = __builtin_amdgcn_make_buffer_rsrc((void*)x, (short)0, 2 * NCH * HW * 2, 0x00020000);
      const unsigned vx = (lane & 15) * (HW * 2) + (lane >> 4) * 16;
      const unsigned sx = (unsigned)((T.b * NCH + T.nt * 64) * HW + T.y0 * 64 + 32 * T.tx) * 2u;
#pragma unroll
      for (int q = 0; q < 6; ++q) { const int k = w * 6 + q; dma1(ldsb + xpiece(0) + k * 1024, vx, rx, sx + (unsigned)(k >> 3) * 16u * HW * 2u + (unsigned)(k & 7) * 128u); }
      wait_vm<6>();
      bar();
#pragma unroll
      for (int q = 0; q < 2; ++q) { const int k = 24 + w * 2 + q; dma1(ldsb + xpiece(24) + (k - 24) * 1024, vx, rx, sx + (unsigned)(k >> 3) * 16u * HW * 2u + (unsigned)(k & 7) * 128u); }
      bar();
      wait_vm<0>();
      bar();
    }
    return;
  }

  const int i16 = lane & 15, kg = lane >> 4;
  {
    constexpr int MODE = 1; (void)MODE;
    if (wid == 0) { STAMP(0); STAMPR(4); }
    const int rho[4] = {4 * p + 1, 4 * p + 2, 4 * p + 3, 4 * p + 4};
    XRows<4> X;
    rows_load<4>(X, rho, ldsb, x, g1, be1, T, ho, lane);
    unsigned short g2c = 0, b2c = 0;
    if (wid == 1) { g2c = g2[T.nt * 64 + lane]; b2c = be2[T.nt * 64 + lane]; }
    wait_vm<0>();
    rows_fetch<4>(X, rho, 4 * p + 1 + (lane >> 5), 4 * p + 3 + (lane >> 5), lds, T, ho, lane);
    rows_stats<4, 4>(X, red, wid, lane);
    if (wid == 1) {
      if (lane < 8) ((float*)(lds + M_ZERO))[lane] = 0.f;
      ((float*)(lds + M_GAM))[lane] = __uint_as_float((unsigned)g2c << 16);
      ((float*)(lds + M_BET))[lane] = __uint_as_float((unsigned)b2c << 16);
    }
    asm volatile("s_waitcnt lgkmcnt(0)" ::: "memory");
    lds_flag_set(lds, 4 + wid, ep);
    if (wid == 0) {
      STAMP(24);
#pragma unroll
      for (int k = 1; k < 4; ++k) lds_flag_wait(lds, 4 + k, ep);
      float tot, m2;
      block_stats(red, tot, m2);
      group_stats(ws, 0, T, tot, m2, ep, lane);
      group_stats_wait(ws, 0, T, tot, m2, ep, red, lane);
      STAMPR(54);
    }
    lds_bar();
    if (wid == 0) { STAMP(25); }
    rows_column<4>(X, lds, wid, lane);
    rows_emit<4>(X, rho, 0, 4, lds, ws, T, wid, ho, lane);
  }
  Comp s;
  s.abase = ldsb + p * 10240 + i16 * 64 + vpos(kg, i16) * 16;
  s.bbase = ldsb + LDS_U + p * 12288 + ho * 2048 + lane * 16;
  {
    constexpr int MODE = 1; (void)MODE;
    if (wid == 0) { STAMP(1); }
    mfma_loop<1>(s, ws, w2, T, ldsb, ep, wid, lane);
    if (wid == 0) { STAMP(2); STAMPR(5); }
    bar();
    if (p == 0) epi1_mfma<0>(s, lds, ws, T, ep, wid, fresh_lane()); else epi1_mfma<1>(s, lds, ws, T, ep, wid, fresh_lane());
  }
  {
    constexpr int MODE = 2; (void)MODE;
    if (wid == 0) { STAMP(0); STAMPR(4); }
      if (wid == 0) { STAMP(1); }
    mfma_loop<2>(s, ws, w2, T, ldsb, ep, wid, lane);
    if (wid == 0) { STAMP(2); STAMPR(5); }
    if (p == 0) epi2_mfma<0>(s, lds, ws, T, out, wid, fresh_lane()); else epi2_mfma<1>(s, lds, ws, T, out, wid, fresh_lane());
  }
  const int le = fresh_lane();
  if (wid == 0 && le < 16) ((unsigned*)(lds + M_RED + 96))[le] = 0u;
  if (bid == 0 && wid == 0 && le == 0) __hip_atomic_store((gu32*)(ws + OFF_EPOCH), ep, RLX_AGENT);
}

extern "C" void kernel_launch(void* const* d_in, const int* in_sizes, int n_in, void* d_out, int out_size, void* d_ws, size_t ws_size,
                              hipStream_t stream) {
  k_main<<<256, 512, 0, stream>>>((const u16*)d_in[0], (const u16*)d_in[1], (const u16*)d_in[2], (const u16*)d_in[3], (const u16*)d_in[4],
                                  (const u16*)d_in[5], (const u16*)d_in[6], (const u16*)d_in[7], (const u16*)d_in[8], (u16*)d_out, (char*)d_ws);
}
```

```cpp
#include <hip/hip_runtime.h>
#include <hip/hip_bf16.h>
#include <stdint.h>
#include <utility>

#define DI __device__ __forceinline__
typedef __bf16 bf16x8 __attribute__((ext_vector_type(8)));
typedef __bf16 bf16x2 __attribute__((ext_vector_type(2)));
typedef float f32x4 __attribute__((ext_vector_type(4)));
typedef float f32x2 __attribute__((ext_vector_type(2)));
typedef unsigned u32x4 __attribute__((ext_vector_type(4)));
typedef unsigned u32x2 __attribute__((ext_vector_type(2)));
typedef unsigned short u16;
#define GAS __attribute__((address_space(1)))
typedef GAS unsigned long long gu64;
typedef GAS unsigned gu32;
#define RLX_AGENT __ATOMIC_RELAXED, __HIP_MEMORY_SCOPE_AGENT

constexpr int NCH = 512, HW = 4096, NEMB = 768;
constexpr float GN_EPS = 1e-5f;
constexpr unsigned SPIN_CAP = 1u << 20;
#define CODE_BYTES (43 * 1024)
#define V_STORE_AUX 16
#define OUT_AUX 16

constexpr size_t U_BYTES = (size_t)8 * 16 * 49152;
constexpr size_t V_BYTES = (size_t)2 * 16 * 4 * 131072;
constexpr size_t OFF_U1 = 0, OFF_U2 = U_BYTES;
constexpr size_t OFF_V1 = 2 * U_BYTES, OFF_V2 = OFF_V1 + V_BYTES;
constexpr size_t OFF_EPOCH = OFF_V2 + V_BYTES;
constexpr size_t OFF_GRAN = OFF_EPOCH + 256;
constexpr size_t OFF_LINES = OFF_GRAN + 8192;
constexpr int W_HALO = 0, W_V1 = 4, W_V2 = 8, W_U1 = 12, W_U2 = 16;
constexpr size_t OFF_EMBP = OFF_LINES + 256 * 128;
constexpr size_t OFF_HALO = OFF_EMBP + 8 * 32 * 512;
constexpr int HALO_STRIDE = 18432;
constexpr size_t OFF_DIAG = OFF_HALO + 256 * HALO_STRIDE;
#define STAMP(i)
#define STAMPR(i)

DI float bf_lo(unsigned v) { return __uint_as_float(v << 16); }
DI float bf_hi(unsigned v) { return __uint_as_float(v & 0xffff0000u); }
DI unsigned pack_bf16(float lo, float hi) {
  f32x2 v = {lo, hi};
  bf16x2 b = __builtin_convertvector(v, bf16x2);
  return __builtin_bit_cast(unsigned, b);
}
DI float silu_f(float y) { return y * __builtin_amdgcn_rcpf(1.f + __builtin_amdgcn_exp2f(y * -1.44269504088896341f)); }
DI f32x2 pk_sub(f32x2 a, f32x2 b) { return __builtin_elementwise_fma(b, f32x2{-1.f, -1.f}, a); }
DI f32x2 lo2(const f32x4& v) { return __builtin_shufflevector(v, v, 0, 1); }
DI f32x2 hi2(const f32x4& v) { return __builtin_shufflevector(v, v, 2, 3); }
DI f32x4 cat2(f32x2 a, f32x2 b) { return __builtin_shufflevector(a, b, 0, 1, 2, 3); }
DI f32x4 sub4(const f32x4& a, const f32x4& b) { return cat2(pk_sub(lo2(a), lo2(b)), pk_sub(hi2(a), hi2(b))); }
DI f32x2 silu_aff2(f32x2 h, f32x2 sa, f32x2 sb) {
  const f32x2 y = __builtin_elementwise_fma(h, sa, sb), t = y * -1.44269504088896341f;
  const f32x2 d = f32x2{__builtin_amdgcn_exp2f(t[0]), __builtin_amdgcn_exp2f(t[1])} + 1.f;
  return y * f32x2{__builtin_amdgcn_rcpf(d[0]), __builtin_amdgcn_rcpf(d[1])};
}
template <int CTRL, int RM = 0xf> DI float dpp0(float v) {
  return __builtin_bit_cast(float, __builtin_amdgcn_update_dpp(0, __builtin_bit_cast(int, v), CTRL, RM, 0xf, true));
}
DI float dpp_prev(float edge, float v) {
  return __builtin_bit_cast(float, __builtin_amdgcn_update_dpp(__builtin_bit_cast(int, edge), __builtin_bit_cast(int, v), 0x111  , 0xf, 0xf, false));
}
DI float dpp_next(float edge, float v) {
  return __builtin_bit_cast(float, __builtin_amdgcn_update_dpp(__builtin_bit_cast(int, edge), __builtin_bit_cast(int, v), 0x101  , 0xf, 0xf, false));
}
DI float wave_sum(float v) {
  v += dpp0<0x111>(v); v += dpp0<0x112>(v); v += dpp0<0x114>(v); v += dpp0<0x118>(v);
  v += dpp0<0x142, 0xa>(v);
  v += dpp0<0x143, 0xc>(v);
  return __builtin_bit_cast(float, __builtin_amdgcn_readlane(__builtin_bit_cast(int, v), 63));
}
DI int vpos(int o, int xt) { return o ^ ((xt >> 1) & 3); }
DI void bar() { asm volatile("s_barrier" ::: "memory"); }
DI int fresh_lane() { int l; asm volatile("v_mbcnt_lo_u32_b32 %0, -1, 0\n\tv_mbcnt_hi_u32_b32 %0, -1, %0" : "=v"(l)); return l; }
DI void lds_bar() { asm volatile("s_waitcnt lgkmcnt(0)\n\ts_barrier" ::: "memory"); }
template <int N> DI void wait_vm() { asm volatile("s_waitcnt vmcnt(%0)" :: "i"(N) : "memory"); }

DI void gn_combine(float s, float q, bool on, float n, float& mean, float& rstd) {
  const float tot = wave_sum(on ? s : 0.f);
  mean = tot * (1.f / (32.f * n));
  const float d = s * (1.f / n) - mean;
  const float m2 = wave_sum(on ? (q + n * d * d) : 0.f);
  rstd = __builtin_amdgcn_rsqf(m2 * (1.f / (32.f * n)) + GN_EPS);
}

constexpr int VSLOT = 20480, NVSLOT = 4;
constexpr int USLOT = 24576, NUSLOT = 3;
constexpr int LDS_U = NVSLOT * VSLOT, LDS_MISC = LDS_U + NUSLOT * USLOT, LDS_TOTAL = 163840;
constexpr int NUNIT = 32;
constexpr int M_EMB = LDS_MISC, M_GAM = LDS_MISC + 256, M_BET = LDS_MISC + 512, M_RED = LDS_MISC + 768, M_ZERO = LDS_MISC + 960, M_RAW = LDS_MISC + 1024, M_COL = LDS_MISC + 3584, M_DUMP = LDS_MISC + 7680;

struct Tile { int bid, b, nt, ty, tx, y0; };
DI int tile_bid(int b, int ty, int tx, int nt) { return (((ty & 3) * 8 + tx * 4 + (nt & 3)) << 3) | (b * 4 + (ty >> 2) * 2 + (nt >> 2)); }
DI int chunk_of(int j, int nt) { return j < 2 ? 2 * nt + j : (j - 2 < 2 * nt ? j - 2 : j); }

struct Loader {
  __amdgpu_buffer_rsrc_t rv, ru;
  unsigned vfirst, vmid, vlast;
  unsigned sv, su;
  unsigned mv, mu;
  int nt;
};
DI void dma1(unsigned m0, unsigned voff, __amdgpu_buffer_rsrc_t r, unsigned soff) {
  asm volatile("s_mov_b32 m0, %0\n\ts_nop 0\n\tbuffer_load_dwordx4 %1, %2, %3 offen lds" :: "s"(m0), "v"(voff), "s"(r), "s"(soff) : "memory");
}
DI void dma1_dev(unsigned m0, unsigned voff, __amdgpu_buffer_rsrc_t r, unsigned soff) {
  asm volatile("s_mov_b32 m0, %0\n\ts_nop 0\n\tbuffer_load_dwordx4 %1, %2, %3 offen sc1 lds" :: "s"(m0), "v"(voff), "s"(r), "s"(soff) : "memory");
}
DI void ld_u(const Loader& L, int k) {
  const unsigned mu = L.mu + (unsigned)(k % NUSLOT) * USLOT, su = L.su + (unsigned)(chunk_of(k >> 1, L.nt) * 2 + (k & 1)) * USLOT;
  asm volatile("s_mov_b32 m0, %0\n\ts_nop 0\n\t"
               "buffer_load_dwordx4 %1, %2, %3 offen sc1 lds\n\t"
               "buffer_load_dwordx4 %1, %2, %3 offen offset:1024 sc1 lds\n\t"
               "buffer_load_dwordx4 %1, %2, %3 offen offset:2048 sc1 lds\n\t"
               "buffer_load_dwordx4 %1, %2, %3 offen offset:3072 sc1 lds"
               :: "s"(mu), "v"(L.vmid), "s"(L.ru), "s"(su) : "memory");
  asm volatile("s_mov_b32 m0, %0\n\ts_nop 0\n\t"
               "buffer_load_dwordx4 %1, %2, %3 offen sc1 lds\n\t"
               "buffer_load_dwordx4 %1, %2, %3 offen offset:1024 sc1 lds"
               :: "s"(mu + 4096), "v"(L.vmid), "s"(L.ru), "s"(su + 4096) : "memory");
}
DI void ld_v(const Loader& L, int k) {
  const unsigned mv = L.mv + (unsigned)(k & 3) * VSLOT, sv = L.sv + (unsigned)(chunk_of(k >> 1, L.nt) * 2 + (k & 1)) * 262144u;
  dma1_dev(mv, L.vfirst, L.rv, sv);
  dma1_dev(mv + 1024, L.vmid, L.rv, sv + 2048);
  dma1_dev(mv + 2048, L.vmid, L.rv, sv + 4096);
  dma1_dev(mv + 3072, L.vmid, L.rv, sv + 6144);
  dma1_dev(mv + 4096, L.vlast, L.rv, sv + 8192);
}

struct Comp {
  f32x4 acc[2][8][2];
  bf16x8 A[10];
  bf16x8 B[2][6];
  unsigned abase, bbase;
  unsigned acur, anxt, bnxt;
};
struct MD { int r, dy, o; };
constexpr MD mdecode(int m) {
  int n = 0;
  for (int r = 0; r < 10; ++r)
    for (int dy = 0; dy < 3; ++dy) {
      if (r - dy < 0 || r - dy > 7) continue;
      for (int o = 0; o < 2; ++o) { if (n == m) return MD{r, dy, o}; ++n; }
    }
  return MD{-1, -1, -1};
}
template <int OFF> DI void lds_rd(bf16x8& dst, unsigned addr) { asm volatile("ds_read_b128 %0, %1 offset:%2" : "=v"(dst) : "v"(addr), "i"(OFF)); }
template <int N> DI void wait_lgkm() { asm volatile("s_waitcnt lgkmcnt(%0)" :: "i"(N) : "memory"); }

constexpr int first_wait(int m) {
  switch (m) { case 2: return 3; case 6: return 3; case 12: return 4; case 18: return 5; case 24: return 6; case 30: return 7;
               case 36: return 8; case 42: return 9; case 46: return 10; default: return -1; }
}
template <int MODE, int E, bool FIRST, int M> DI void mstep(Comp& s) {
  constexpr MD d = mdecode(M);
  if constexpr (first_wait(M) >= 0) wait_lgkm<first_wait(M)>();
  if constexpr (FIRST && d.dy == 0) {
    if constexpr (MODE == 1) asm volatile("v_mfma_f32_16x16x32_bf16 %0, %1, %2, 0" : "=&v"(s.acc[E][d.r][d.o]) : "v"(s.B[E][d.o]), "v"(s.A[d.r]));
    else                     asm volatile("v_mfma_f32_16x16x32_bf16 %0, %1, %2, 0" : "=&v"(s.acc[E][d.r][d.o]) : "v"(s.A[d.r]), "v"(s.B[E][d.o]));
  } else {
    if constexpr (MODE == 1) asm volatile("v_mfma_f32_16x16x32_bf16 %0, %1, %2, %0" : "+v"(s.acc[E][d.r - d.dy][d.o]) : "v"(s.B[E][d.dy * 2 + d.o]), "v"(s.A[d.r]));
    else                     asm volatile("v_mfma_f32_16x16x32_bf16 %0, %1, %2, %0" : "+v"(s.acc[E][d.r - d.dy][d.o]) : "v"(s.A[d.r]), "v"(s.B[E][d.dy * 2 + d.o]));
  }
  if constexpr (M % 3 == 0) {
    constexpr int j = M / 3;
    if constexpr (j < 6) lds_rd<(4 + j) * 1024>(s.A[4 + j], s.acur);
    else if constexpr (j == 6) lds_rd<0>(s.B[E ^ 1][0], s.bnxt);
    else if constexpr (j == 7) lds_rd<1024>(s.B[E ^ 1][1], s.bnxt);
    else if constexpr (j == 8) lds_rd<0>(s.A[0], s.anxt);
    else if constexpr (j == 9) lds_rd<4096>(s.B[E ^ 1][2], s.bnxt);
    else if constexpr (j == 10) lds_rd<4096 + 1024>(s.B[E ^ 1][3], s.bnxt);
    else if constexpr (j == 11) lds_rd<8192>(s.B[E ^ 1][4], s.bnxt);
    else if constexpr (j == 12) lds_rd<8192 + 1024>(s.B[E ^ 1][5], s.bnxt);
    else lds_rd<(j - 12) * 1024>(s.A[j - 12], s.anxt);
  }
}
template <int MODE, int E, bool FIRST, int... Ms> DI void msteps(Comp& s, std::integer_sequence<int, Ms...>) { (mstep<MODE, E, FIRST, Ms>(s), ...); }

template <int MODE, int E, bool FIRST = false> DI void unit(Comp& s, unsigned& voff, unsigned& uoff) {
  bar();
  s.acur = s.anxt;
  voff = (voff + VSLOT) == NVSLOT * VSLOT ? 0u : voff + VSLOT;
  s.anxt = s.abase + voff;
  uoff = (uoff + USLOT) == NUSLOT * USLOT ? 0u : uoff + USLOT;
  s.bnxt = s.bbase + uoff;
  msteps<MODE, E, FIRST>(s, std::make_integer_sequence<int, 48>{});
  wait_lgkm<3>();
}

struct Affine { f32x2 sa[4], sb[4]; };
DI void row_transform(const f32x4 (&ye)[2], const f32x4 (&yo)[2], const f32x4 (&hl)[2], const f32x4 (&hr)[2], const Affine& af, u32x4 (&o)[4]) {
#pragma unroll
  for (int q = 0; q < 4; ++q) {
    const f32x2 he = (q & 1) ? hi2(ye[q >> 1]) : lo2(ye[q >> 1]), ho2 = (q & 1) ? hi2(yo[q >> 1]) : lo2(yo[q >> 1]);
    const f32x2 l2 = (q & 1) ? hi2(hl[q >> 1]) : lo2(hl[q >> 1]), r2 = (q & 1) ? hi2(hr[q >> 1]) : lo2(hr[q >> 1]);
    const f32x2 d1 = silu_aff2(he, af.sa[q], af.sb[q]);
    const f32x2 d2 = silu_aff2(ho2, af.sa[q], af.sb[q]);
    const f32x2 d0 = {dpp_prev(l2[0], d2[0]), dpp_prev(l2[1], d2[1])}, d3 = {dpp_next(r2[0], d1[0]), dpp_next(r2[1], d1[1])};
    const f32x2 t0 = pk_sub(d0, d2), t1 = d1 + d2, t2 = pk_sub(d2, d1), t3 = pk_sub(d1, d3);
    o[0][q] = pack_bf16(t0[0], t0[1]); o[1][q] = pack_bf16(t1[0], t1[1]); o[2][q] = pack_bf16(t2[0], t2[1]); o[3][q] = pack_bf16(t3[0], t3[1]);
  }
}
DI void halo_ops(const char* lds, const float* c, int tx, int kg, f32x4 (&hl)[2], f32x4 (&hr)[2]) {
  const float* z = (const float*)(lds + M_ZERO);
  const float* pl = tx == 1 ? c + 8 * kg : z;
  const float* pr = tx == 0 ? c + 8 * kg : z;
  hl[0] = *(const f32x4*)pl; hl[1] = *(const f32x4*)(pl + 4);
  hr[0] = *(const f32x4*)pr; hr[1] = *(const f32x4*)(pr + 4);
}

DI f32x4 add4_here(const f32x4& a, const f32x4& b) {
  f32x2 lo, hi;
  asm volatile("v_pk_add_f32 %0, %1, %2" : "=v"(lo) : "v"(lo2(a)), "v"(lo2(b)));
  asm volatile("v_pk_add_f32 %0, %1, %2" : "=v"(hi) : "v"(hi2(a)), "v"(hi2(b)));
  return cat2(lo, hi);
}
template <int P> DI void exchange(const Comp& s, char* lds, int wid, int lane, f32x4 (&ye)[4][2], f32x4 (&yo)[4][2]) {
  char* xb = lds + wid * 16384 + lane * 16;
  const char* rb = lds + (wid ^ 1) * 16384 + lane * 16;
#pragma unroll
  for (int r = 0; r < 4; ++r)
#pragma unroll
    for (int o = 0; o < 2; ++o) {
      if constexpr (P == 0) {
        *(f32x4*)(xb + (r * 4 + o * 2) * 1024) = add4_here(s.acc[0][4 + r][o], s.acc[1][4 + r][o]);
        *(f32x4*)(xb + (r * 4 + o * 2 + 1) * 1024) = s.acc[1][4 + r][o];
      } else {
        *(f32x4*)(xb + (r * 4 + o * 2) * 1024) = s.acc[0][r][o];
        *(f32x4*)(xb + (r * 4 + o * 2 + 1) * 1024) = add4_here(s.acc[0][r][o], s.acc[1][r][o]);
      }
      __builtin_amdgcn_sched_barrier(0);
    }
  lds_bar();
#pragma unroll
  for (int o = 0; o < 2; ++o) {
    f32x4 r0[4], r1[4];
#pragma unroll
    for (int r = 0; r < 4; ++r) { r0[r] = *(const f32x4*)(rb + (r * 4 + o * 2) * 1024); r1[r] = *(const f32x4*)(rb + (r * 4 + o * 2 + 1) * 1024); }
#pragma unroll
    for (int r = 0; r < 4; ++r) {
      if constexpr (P == 0) { ye[r][o] = add4_here(s.acc[0][r][o], s.acc[1][r][o]) + r0[r]; yo[r][o] = sub4(s.acc[1][r][o], r1[r]); }
      else                  { ye[r][o] = r0[r] + s.acc[0][4 + r][o]; yo[r][o] = sub4(r1[r], add4_here(s.acc[0][4 + r][o], s.acc[1][4 + r][o])); }
    }
    __builtin_amdgcn_sched_barrier(0);
  }
}

DI void load_affine(Affine& af, const char* lds, int ho, int kg) {
  const float* red = (const float*)(lds + M_RED);
  const float mean = red[16], rstd = red[17];
  const f32x4 g0 = *(const f32x4*)(lds + M_GAM + (32 * ho + 8 * kg) * 4), g1 = *(const f32x4*)(lds + M_GAM + (32 * ho + 8 * kg) * 4 + 16);
  const f32x4 b0 = *(const f32x4*)(lds + M_BET + (32 * ho + 8 * kg) * 4), b1 = *(const f32x4*)(lds + M_BET + (32 * ho + 8 * kg) * 4 + 16);
#pragma unroll
  for (int q = 0; q < 4; ++q) {
    const f32x2 g = q == 0 ? lo2(g0) : q == 1 ? hi2(g0) : q == 2 ? lo2(g1) : hi2(g1);
    const f32x2 bb = q == 0 ? lo2(b0) : q == 1 ? hi2(b0) : q == 2 ? lo2(b1) : hi2(b1);
    af.sa[q] = g * rstd; af.sb[q] = bb - af.sa[q] * mean;
  }
}
DI void set_tag(char* ws, int bid, int word, unsigned ep, int lane) {
  if (lane == 0) __hip_atomic_store((gu32*)(ws + OFF_LINES) + bid * 32 + word, ep, RLX_AGENT);
}
template <int NQ> DI void wait_tags(char* ws, int who, int w0, bool need, unsigned ep) {
  const __amdgpu_buffer_rsrc_t rl = __builtin_amdgcn_make_buffer_rsrc((void*)(ws + OFF_LINES), (short)0, 256 * 128, 0x00020000);
  for (unsigned spins = 0;; ++spins) {
    bool ok = true;
#pragma unroll
    for (int q = 0; q < NQ; ++q) {
      const u32x4 v = __builtin_amdgcn_raw_buffer_load_b128(rl, who * 128 + w0 * 4 + q * 16, 0, 16);
      ok = ok && v[0] == ep && v[1] == ep && v[2] == ep && v[3] == ep;
    }
    if (__all(!need || ok)) break;
    if (spins > SPIN_CAP) break;
    __builtin_amdgcn_s_sleep(2);
    asm volatile("" ::: "memory");
  }
  asm volatile("" ::: "memory");
}
DI unsigned lds_key(unsigned ep, int idx) { return (ep * 16u + (unsigned)idx) * 0x9E3779B1u + 0x3C6EF372u; }
DI void lds_flag_set(char* lds, int idx, unsigned ep) {
  const unsigned key = lds_key(ep, idx);
  __hip_atomic_store((__attribute__((address_space(3))) unsigned*)(lds + M_RED + 96 + idx * 4), key, __ATOMIC_RELAXED, __HIP_MEMORY_SCOPE_WORKGROUP);
}
DI void lds_flag_wait(char* lds, int idx, unsigned ep) {
  const unsigned key = lds_key(ep, idx);
  for (unsigned spins = 0; spins < (SPIN_CAP << 4); ++spins) {
    const unsigned v = __hip_atomic_load((__attribute__((address_space(3))) unsigned*)(lds + M_RED + 96 + idx * 4), __ATOMIC_RELAXED, __HIP_MEMORY_SCOPE_WORKGROUP);
    if (__builtin_amdgcn_readfirstlane(v) == key) break;
    __builtin_amdgcn_s_sleep(1);
  }
  asm volatile("" ::: "memory");
}
DI bool lds_flag_test(char* lds, int idx, unsigned ep) {
  const unsigned v = __hip_atomic_load((__attribute__((address_space(3))) unsigned*)(lds + M_RED + 96 + idx * 4), __ATOMIC_RELAXED, __HIP_MEMORY_SCOPE_WORKGROUP);
  return __builtin_amdgcn_readfirstlane(v) == lds_key(ep, idx);
}
DI void block_stats(const float* red, float& tot, float& m2) {
  const float s0 = red[0], s1 = red[1], s2 = red[2], s3 = red[3];
  tot = (s0 + s1) + (s2 + s3);
  const float bm = tot * (1.f / 16384.f);
  const float d0 = s0 * (1.f / 4096.f) - bm, d1 = s1 * (1.f / 4096.f) - bm, d2 = s2 * (1.f / 4096.f) - bm, d3 = s3 * (1.f / 4096.f) - bm;
  m2 = (red[8] + red[9]) + (red[10] + red[11]) + 4096.f * ((d0 * d0 + d1 * d1) + (d2 * d2 + d3 * d3));
}
DI int gran_slot(const Tile& T) { return T.ty * 4 + T.tx * 2 + (T.nt & 1); }
DI void group_stats(char* ws, int conv, const Tile& T, float tot, float m2, unsigned ep, int lane) {
  gu64* gran = (gu64*)(ws + OFF_GRAN) + (conv * 8 + T.b * 4 + (T.nt >> 1)) * 64;
  if (lane < 2) {
    const float val = lane == 0 ? tot : m2;
    __hip_atomic_store(gran + gran_slot(T) * 2 + lane, ((unsigned long long)ep << 32) | (unsigned long long)__float_as_uint(val), RLX_AGENT);
  }
}
DI void group_stats_wait(char* ws, int conv, const Tile& T, float tot, float m2, unsigned ep, float* red, int lane) {
  gu64* gran = (gu64*)(ws + OFF_GRAN) + (conv * 8 + T.b * 4 + (T.nt >> 1)) * 64;
  const bool own = (lane >> 1) == gran_slot(T);
  unsigned long long g;
  for (unsigned spins = 0;; ++spins) {
    g = __hip_atomic_load(gran + lane, RLX_AGENT);
    if (__all(own || (unsigned)(g >> 32) == ep)) break;
    if (spins > SPIN_CAP) break;
    __builtin_amdgcn_s_sleep(1);
  }
  const float val = own ? ((lane & 1) ? m2 : tot) : __uint_as_float((unsigned)g);
  const float qk = dpp0<0x101>(val);
  float mean, rstd;
  gn_combine(val, qk, !(lane & 1), 16384.f, mean, rstd);
  if (lane == 0) { red[16] = mean; red[17] = rstd; }
}

DI void put_row(char* lds, __amdgpu_buffer_rsrc_t rv, const Tile& T, int ho, int rho, bool publish, int i16, int kg, const u32x4 (&o)[4]) {
  const int pos = i16 * 64 + vpos(kg, i16) * 16;
  char* d = lds + 2 * ho * VSLOT + rho * 1024 + pos;
#pragma unroll
  for (int xi = 0; xi < 4; ++xi) *(u32x4*)(d + (xi & 1) * VSLOT + (xi >> 1) * 10240) = o[xi];
  if (publish) {
    const int so = (T.b * 16 + 2 * T.nt + ho) * 524288 + (T.y0 - 1 + rho) * 2048 + T.tx * 1024;
#pragma unroll
    for (int xi = 0; xi < 4; ++xi)
      __builtin_amdgcn_raw_buffer_store_b128(o[xi], rv, pos + so + (xi & 1) * 262144 + (xi >> 1) * 131072, 0, V_STORE_AUX);
  }
}

template <int NR> struct XRows { unsigned v[NR][8]; unsigned short col[2]; u32x4 gv, bv; unsigned short gc, bc; };
DI int clamp_y(int y) { return min(max(y, 0), 63); }
template <int NR> DI void rows_stage(const int (&rho)[NR], int k0, int k1, unsigned ldsb, const u16* __restrict__ x, const Tile& T, int ho, int lane) {
  const __amdgpu_buffer_rsrc_t rx = __builtin_amdgcn_make_buffer_rsrc((void*)x, (short)0, 2 * NCH * HW * 2, 0x00020000);
  const int cl = lane >> 3;
#pragma unroll
  for (int k = 0; k < NR; ++k)
#pragma unroll
    for (int g = 0; g < 4; ++g) {
      if (k < k0 || k >= k1) continue;
      const unsigned vo = (unsigned)(cl * (HW * 2) + (((lane & 7) - 4 * (g & 1)) & 7) * 16);
      const unsigned so = (unsigned)(((T.b * NCH + T.nt * 64 + 32 * ho + 8 * g) * HW + clamp_y(T.y0 - 1 + rho[k]) * 64) * 2);
      dma1(ldsb + (rho[k] * 2 + ho) * 4096 + g * 1024, vo, rx, so);
    }
}
template <int NR> DI void rows_params(XRows<NR>& X, const u16* __restrict__ gam, const u16* __restrict__ bet, const Tile& T, int ho, int lane) {
  const int kg = lane >> 4, chl = lane & 31, ch = T.nt * 64 + 32 * ho;
  X.gv = *(const u32x4*)(gam + ch + 8 * kg); X.bv = *(const u32x4*)(bet + ch + 8 * kg);
  X.gc = gam[ch + chl]; X.bc = bet[ch + chl];
}
template <int NR> DI void rows_fetch(XRows<NR>& X, const int (&rho)[NR], int k0, int k1, const char* lds, const Tile& T, int ho, int lane) {
  const int i16 = lane & 15, kg = lane >> 4;
  const int pos = (((T.tx * 4 + (i16 >> 2)) + 4 * (kg & 1)) & 7) * 16 + (i16 & 3) * 4;
#pragma unroll
  for (int k = 0; k < NR; ++k)
#pragma unroll
    for (int j = 0; j < 8; ++j)
      if (k >= k0 && k < k1) X.v[k][j] = *(const unsigned*)(lds + (rho[k] * 2 + ho) * 4096 + (8 * kg + j) * 128 + pos);
}
template <int NR> DI void rows_fetch_col(XRows<NR>& X, int rc0, int rc1, const char* lds, const Tile& T, int ho, int lane) {
  const int chl = lane & 31;
  const int xc = T.tx == 0 ? 32 : 31;
  const int cpos = chl * 128 + (((xc >> 3) + 4 * ((chl >> 3) & 1)) & 7) * 16 + (xc & 7) * 2;
  X.col[0] = *(const unsigned short*)(lds + (rc0 * 2 + ho) * 4096 + cpos);
  X.col[1] = *(const unsigned short*)(lds + (rc1 * 2 + ho) * 4096 + cpos);
}
template <int NR, int NI> DI void rows_stats(const XRows<NR>& X, float* red, int wid, int lane) {
  const float piv = __builtin_bit_cast(float, __builtin_amdgcn_readfirstlane(X.v[0][0] << 16));
  f32x2 sv = {0.f, 0.f}, qv = {0.f, 0.f};
#pragma unroll
  for (int k = 0; k < NI; ++k)
#pragma unroll
    for (int j = 0; j < 8; ++j) {
      const f32x2 d = f32x2{bf_lo(X.v[k][j]), bf_hi(X.v[k][j])} - piv;
      sv += d; qv = __builtin_elementwise_fma(d, d, qv);
    }
  const float s = wave_sum(sv[0] + sv[1]), q = wave_sum(qv[0] + qv[1]);
  if (lane == 0) { red[wid] = s + piv * (float)(NI * 1024); red[8 + wid] = q - s * s * (1.f / (float)(NI * 1024)); }
}
template <int NR> DI void rows_emit(const XRows<NR>& X, const int (&rho)[NR], int k0, int k1, char* lds, char* ws, const Tile& T, int wid, int ho, int lane) {
  const int i16 = lane & 15, kg = lane >> 4;
  const float* red = (const float*)(lds + M_RED);
  const float mean = red[16], rstd = red[17];
  float sa[8], sb[8];
#pragma unroll
  for (int j = 0; j < 8; ++j) {
    sa[j] = rstd * ((j & 1) ? bf_hi(X.gv[j >> 1]) : bf_lo(X.gv[j >> 1]));
    sb[j] = ((j & 1) ? bf_hi(X.bv[j >> 1]) : bf_lo(X.bv[j >> 1])) - mean * sa[j];
  }
  const float* col = (const float*)(lds + M_COL + wid * 512);
  const __amdgpu_buffer_rsrc_t rv = __builtin_amdgcn_make_buffer_rsrc((void*)(ws + OFF_V1), (short)0, (int)V_BYTES, 0x00020000);
#pragma unroll
  for (int k = 0; k < NR; ++k) {
    if (k < k0 || k >= k1) continue;
    f32x4 hl[2], hr[2];
    halo_ops(lds, col + k * 32, T.tx, kg, hl, hr);
    float t[4][8];
#pragma unroll
    for (int j = 0; j < 8; ++j) {
      const f32x2 a2 = {sa[j], sa[j]}, b2 = {sb[j], sb[j]};
      const f32x2 d = silu_aff2(f32x2{bf_lo(X.v[k][j]), bf_hi(X.v[k][j])}, a2, b2);
      t[0][j] = dpp_prev(hl[j >> 2][j & 3], d[1]) - d[1];
      t[1][j] = d[0] + d[1];
      t[2][j] = d[1] - d[0];
      t[3][j] = d[0] - dpp_next(hr[j >> 2][j & 3], d[0]);
    }
    u32x4 o[4];
#pragma unroll
    for (int xi = 0; xi < 4; ++xi)
      o[xi] = u32x4{pack_bf16(t[xi][0], t[xi][1]), pack_bf16(t[xi][2], t[xi][3]), pack_bf16(t[xi][4], t[xi][5]), pack_bf16(t[xi][6], t[xi][7])};
    const int y = T.y0 - 1 + rho[k];
    const bool halo_row = NR == 1;
    if (halo_row && (y < 0 || y > 63)) { o[0] = o[1] = o[2] = o[3] = u32x4{0u, 0u, 0u, 0u}; }
    put_row(lds, rv, T, ho, rho[k], !halo_row, i16, kg, o);
    __builtin_amdgcn_sched_barrier(0);
  }
}
template <int NR> DI void rows_column(const XRows<NR>& X, char* lds, int wid, int lane) {
  const float* red = (const float*)(lds + M_RED);
  const float mean = red[16], rstd = red[17];
  float* col = (float*)(lds + M_COL + wid * 512);
  const float sa = rstd * __uint_as_float((unsigned)X.gc << 16), sb = __uint_as_float((unsigned)X.bc << 16) - mean * sa;
  col[lane] = silu_f(__uint_as_float((unsigned)X.col[0] << 16) * sa + sb);
  if constexpr (NR > 2) col[64 + lane] = silu_f(__uint_as_float((unsigned)X.col[1] << 16) * sa + sb);
}

DI int u_row(int conv, int of, int i) { return conv == 0 ? (32 * (of >> 1) + 8 * (i >> 2) + 4 * (of & 1) + (i & 3)) : (16 * of + i); }
DI void u_load(u32x4 (&d)[9], const u16* __restrict__ w, int conv, const Tile& T, int uidx, int of, int lane) {
  const int i = lane & 15, kg = lane >> 4;
  const u16* src = w + ((size_t)(T.nt * 64 + u_row(conv, of, i)) * 512 + 32 * (uidx >> 1) + 8 * kg) * 9;
  asm volatile("global_load_dwordx4 %0, %9, off\n\tglobal_load_dwordx4 %1, %9, off offset:16\n\tglobal_load_dwordx4 %2, %9, off offset:32\n\t"
               "global_load_dwordx4 %3, %9, off offset:48\n\tglobal_load_dwordx4 %4, %9, off offset:64\n\tglobal_load_dwordx4 %5, %9, off offset:80\n\t"
               "global_load_dwordx4 %6, %9, off offset:96\n\tglobal_load_dwordx4 %7, %9, off offset:112\n\tglobal_load_dwordx4 %8, %9, off offset:128"
               : "=&v"(d[0]), "=&v"(d[1]), "=&v"(d[2]), "=&v"(d[3]), "=&v"(d[4]), "=&v"(d[5]), "=&v"(d[6]), "=&v"(d[7]), "=&v"(d[8])
               : "v"(src) : "memory");
}
DI void u_landed(u32x4 (&d)[9]) {
  asm volatile("" : "+v"(d[0]), "+v"(d[1]), "+v"(d[2]), "+v"(d[3]), "+v"(d[4]), "+v"(d[5]), "+v"(d[6]), "+v"(d[7]), "+v"(d[8]) :: "memory");
}
DI void u_stage(unsigned ldsdst, const u16* __restrict__ w, int conv, const Tile& T, int uidx, int of, int lane) {
  const __amdgpu_buffer_rsrc_t rw = __builtin_amdgcn_make_buffer_rsrc((void*)w, (short)0, 512 * 512 * 9 * 2, 0x00020000);
#pragma unroll
  for (int k = 0; k < 9; ++k) {
    const int t = k * 64 + lane, row = t / 36, c16 = t - row * 36;
    dma1(ldsdst + k * 1024, (unsigned)(((T.nt * 64 + u_row(conv, of, row)) * 512 + 32 * (uidx >> 1)) * 18 + c16 * 16), rw, 0u);
  }
}
DI void u_touch(unsigned ldsb, const u16* __restrict__ w, int conv, const Tile& T, int uidx, int of, int lane) {
  const __amdgpu_buffer_rsrc_t rw = __builtin_amdgcn_make_buffer_rsrc((void*)w, (short)0, 512 * 512 * 9 * 2, 0x00020000);
#pragma unroll
  for (int h = 0; h < 2; ++h) {
    const int t = min(64 * h + lane, 79), row = t / 5;
    const unsigned vo = (unsigned)(((T.nt * 64 + u_row(conv, of, row)) * 512 + 32 * (uidx >> 1)) * 18 + (t - row * 5) * 128);
    asm volatile("s_mov_b32 m0, %0\n\ts_nop 0\n\tbuffer_load_dword %1, %2, 0 offen lds" :: "s"(ldsb + M_DUMP), "v"(vo), "s"(rw) : "memory");
  }
}
DI void u_fetch(u32x4 (&d)[9], const char* stage, int lane) {
#pragma unroll
  for (int k = 0; k < 9; ++k) d[k] = *(const u32x4*)(stage + (lane & 15) * 576 + (lane >> 4) * 144 + k * 16);
}
DI void u_emit(const u32x4 (&d)[9], char* ws, int conv, const Tile& T, int uidx, int of, int lane) {
  const __amdgpu_buffer_rsrc_t ru = __builtin_amdgcn_make_buffer_rsrc((void*)(ws + (conv == 0 ? OFF_U1 : OFF_U2)), (short)0, (int)U_BYTES, 0x00020000);
  const int dst = T.nt * 786432 + uidx * USLOT + of * 1024 + lane * 16;
  const float sg = (uidx & 1) ? 1.f : -1.f;
#pragma unroll
  for (int dy = 0; dy < 3; ++dy) {
    float ua[8], ub[8];
#pragma unroll
    for (int j = 0; j < 8; ++j) {
      float g[3];
#pragma unroll
      for (int dx = 0; dx < 3; ++dx) {
        const int e = 9 * j + 3 * dy + dx;
        const unsigned dw = d[e >> 3][(e >> 1) & 3];
        g[dx] = (e & 1) ? bf_hi(dw) : bf_lo(dw);
      }
      const float mid = 0.5f * (g[0] + sg * g[1] + g[2]);
      ua[j] = (uidx & 1) ? mid : g[0];
      ub[j] = (uidx & 1) ? g[2] : mid;
    }
    const u32x4 oa = {pack_bf16(ua[0], ua[1]), pack_bf16(ua[2], ua[3]), pack_bf16(ua[4], ua[5]), pack_bf16(ua[6], ua[7])};
    const u32x4 ob = {pack_bf16(ub[0], ub[1]), pack_bf16(ub[2], ub[3]), pack_bf16(ub[4], ub[5]), pack_bf16(ub[6], ub[7])};
    __builtin_amdgcn_raw_buffer_store_b128(oa, ru, dst + dy * 4096, 0, V_STORE_AUX);
    __builtin_amdgcn_raw_buffer_store_b128(ob, ru, dst + 12288 + dy * 4096, 0, V_STORE_AUX);
  }
}

DI void epi1_mfma(const Comp& s, char* lds, char* ws, const Tile& T, unsigned ep, int wid, int lane) {
  const int P = wid & 1;
  constexpr int MODE = 1; (void)MODE;
  const int i16 = lane & 15, kg = lane >> 4, ho = wid >> 1;
  f32x4 ye[4][2], yo[4][2];
  if (P == 0) exchange<0>(s, lds, wid, lane, ye, yo); else exchange<1>(s, lds, wid, lane, ye, yo);
  if (wid == 0) { STAMP(12); }
  {
    const f32x4 e0 = *(const f32x4*)(lds + M_EMB + (32 * ho + 8 * kg) * 4), e1 = *(const f32x4*)(lds + M_EMB + (32 * ho + 8 * kg) * 4 + 16);
#pragma unroll
    for (int r = 0; r < 4; ++r) { ye[r][0] += e0; ye[r][1] += e1; yo[r][0] += e0; yo[r][1] += e1; }
  }
  const __amdgpu_buffer_rsrc_t rh = __builtin_amdgcn_make_buffer_rsrc((void*)(ws + OFF_HALO), (short)0, 256 * HALO_STRIDE, 0x00020000);
  float* red = (float*)(lds + M_RED);
  const int hb = T.bid * HALO_STRIDE;
#pragma unroll
  for (int f = 0; f < 2; ++f) {
    __builtin_amdgcn_raw_buffer_store_b128(__builtin_bit_cast(u32x4, P == 0 ? ye[0][f] : ye[3][f]), rh, hb + P * 8192 + ((ho * 2 + f) * 2) * 1024 + lane * 16, 0, 16);
    __builtin_amdgcn_raw_buffer_store_b128(__builtin_bit_cast(u32x4, P == 0 ? yo[0][f] : yo[3][f]), rh, hb + P * 8192 + ((ho * 2 + f) * 2 + 1) * 1024 + lane * 16, 0, 16);
  }
  if (i16 == (T.tx == 0 ? 15 : 0)) {
#pragma unroll
    for (int r = 0; r < 4; ++r)
#pragma unroll
      for (int f = 0; f < 2; ++f)
        __builtin_amdgcn_raw_buffer_store_b128(__builtin_bit_cast(u32x4, T.tx == 0 ? yo[r][f] : ye[r][f]), rh,
                                               hb + 16384 + (4 * P + r) * 256 + (32 * ho + 8 * kg + 4 * f) * 4, 0, 16);
  }
  {
    const float piv = __builtin_bit_cast(float, __builtin_amdgcn_readfirstlane(__builtin_bit_cast(int, ye[0][0][0])));
    f32x4 sv = {0.f, 0.f, 0.f, 0.f}, qv = {0.f, 0.f, 0.f, 0.f};
#pragma unroll
    for (int r = 0; r < 4; ++r)
#pragma unroll
      for (int f = 0; f < 2; ++f) {
        const f32x4 d0 = ye[r][f] - piv, d1 = yo[r][f] - piv;
        sv += d0 + d1;
        qv = __builtin_elementwise_fma(d0, d0, qv);
        qv = __builtin_elementwise_fma(d1, d1, qv);
      }
    const float sum = wave_sum((sv[0] + sv[1]) + (sv[2] + sv[3])), q = wave_sum((qv[0] + qv[1]) + (qv[2] + qv[3]));
    if (lane == 0) { red[wid] = sum + piv * 4096.f; red[8 + wid] = q - sum * sum * (1.f / 4096.f); }
  }
  lds_bar();
  if (wid == 0) { STAMP(13); }
  wait_vm<0>();
  set_tag(ws, T.bid, W_HALO + wid, ep, lane);
  const int chl = lane & 31, half = lane >> 5;
  const float gam_c = ((const float*)(lds + M_GAM))[32 * ho + chl], bet_c = ((const float*)(lds + M_BET))[32 * ho + chl];
  lds_bar();
  if (wid == 0) { STAMP(11); }
  Affine af;
  load_affine(af, lds, ho, kg);
  float* col = (float*)(lds + M_COL + wid * 512);
  {
    const float* raw = (const float*)(lds + M_RAW);
    const float sa = red[17] * gam_c, sb = bet_c - red[16] * sa;
    col[lane] = silu_f(raw[(4 * P + 1 + half) * 64 + 32 * ho + chl] * sa + sb);
    col[64 + lane] = silu_f(raw[(4 * P + 3 + half) * 64 + 32 * ho + chl] * sa + sb);
  }
  if (wid == 0) { STAMP(18); }
  const __amdgpu_buffer_rsrc_t rv = __builtin_amdgcn_make_buffer_rsrc((void*)(ws + OFF_V2), (short)0, (int)V_BYTES, 0x00020000);
#pragma unroll
  for (int k = 0; k < 4; ++k) {
    f32x4 hl[2], hr[2];
    halo_ops(lds, col + k * 32, T.tx, kg, hl, hr);
    u32x4 o[4];
    row_transform(ye[k], yo[k], hl, hr, af, o);
    put_row(lds, rv, T, ho, 4 * P + k + 1, true, i16, kg, o);
    __builtin_amdgcn_sched_barrier(0);
  }
  if (wid == 0) { STAMP(19); }
}

DI void epi1_loader(const Loader& L, const u16* __restrict__ w2, int uprod, int uidx, char* lds, char* ws, const Tile& T, unsigned ep, int wid, int lane) {
  constexpr int MODE = 1; (void)MODE;
  const int i16 = lane & 15, kg = lane >> 4, p = wid & 1, ho = (wid >> 1) & 1;
  const __amdgpu_buffer_rsrc_t rh = __builtin_amdgcn_make_buffer_rsrc((void*)(ws + OFF_HALO), (short)0, 256 * HALO_STRIDE, 0x00020000);
  float* red = (float*)(lds + M_RED);
  {
    u32x4 dw2[9];
    u_load(dw2, w2, 1, T, uidx, wid - 4, lane);
    lds_bar();
    bar();
    wait_vm<0>();
    u_landed(dw2);
    u_emit(dw2, ws, 1, T, uidx, wid - 4, lane);
  }
  bar();
  float tot = 0.f, m2 = 0.f;
  if (wid == 4) {
    block_stats(red, tot, m2);
    group_stats(ws, 1, T, tot, m2, ep, lane);
    STAMP(14); STAMPR(16);
  }
  wait_vm<0>();
  set_tag(ws, T.bid, W_U2 + (wid - 4), ep, lane);
  if (wid == 4) {
    group_stats_wait(ws, 1, T, tot, m2, ep, red, lane);
    STAMP(15); STAMPR(17);
  }
  const int tyn = p == 0 ? T.ty - 1 : T.ty + 1;
  const bool have_v = tyn >= 0 && tyn < 8;
  const int bid_v = tile_bid(T.b, have_v ? tyn : T.ty, T.tx, T.nt);
  f32x4 ve[2], vo[2];
  const int hbv = bid_v * HALO_STRIDE + (p == 0 ? 8192 : 0) + ho * 4096 + lane * 16;
  if (wid != 4) {
    wait_tags<1>(ws, bid_v, W_HALO, have_v, ep);
#pragma unroll
    for (int f = 0; f < 2; ++f) {
      ve[f] = __builtin_bit_cast(f32x4, __builtin_amdgcn_raw_buffer_load_b128(rh, hbv + f * 2048, 0, 16));
      vo[f] = __builtin_bit_cast(f32x4, __builtin_amdgcn_raw_buffer_load_b128(rh, hbv + f * 2048 + 1024, 0, 16));
    }
  }
  if (wid == 5) {
    const int nb = T.bid ^ 32;
    wait_tags<1>(ws, nb, W_HALO, true, ep);
    const u32x4 c0 = __builtin_amdgcn_raw_buffer_load_b128(rh, nb * HALO_STRIDE + 16384 + lane * 16, 0, 16);
    const u32x4 c1 = __builtin_amdgcn_raw_buffer_load_b128(rh, nb * HALO_STRIDE + 16384 + 1024 + lane * 16, 0, 16);
    *(u32x4*)(lds + M_RAW + 256 + lane * 16) = c0;
    *(u32x4*)(lds + M_RAW + 256 + 1024 + lane * 16) = c1;
  } else if (wid >= 6) {
    const int tyd = wid == 6 ? T.ty - 1 : T.ty + 1;
    float cv = 0.f;
    if (tyd >= 0 && tyd < 8) {
      const int nb = tile_bid(T.b, tyd, T.tx ^ 1, T.nt);
      wait_tags<1>(ws, nb, W_HALO, true, ep);
      cv = __uint_as_float(__builtin_amdgcn_raw_buffer_load_b32(rh, nb * HALO_STRIDE + 16384 + (wid == 6 ? 7 : 0) * 256 + lane * 4, 0, 16));
    }
    ((float*)(lds + M_RAW))[(wid == 6 ? 0 : 9) * 64 + lane] = cv;
  }
  const int chl = lane & 31;
  const float gam_c = ((const float*)(lds + M_GAM))[32 * ho + chl], bet_c = ((const float*)(lds + M_BET))[32 * ho + chl];
  lds_bar();
  if (wid == 4) {
    wait_tags<1>(ws, bid_v, W_HALO, have_v, ep);
#pragma unroll
    for (int f = 0; f < 2; ++f) {
      ve[f] = __builtin_bit_cast(f32x4, __builtin_amdgcn_raw_buffer_load_b128(rh, hbv + f * 2048, 0, 16));
      vo[f] = __builtin_bit_cast(f32x4, __builtin_amdgcn_raw_buffer_load_b128(rh, hbv + f * 2048 + 1024, 0, 16));
    }
  }
  Affine af;
  load_affine(af, lds, ho, kg);
  float* col = (float*)(lds + M_COL + wid * 512);
  {
    const float* raw = (const float*)(lds + M_RAW);
    const float sa = red[17] * gam_c, sb = bet_c - red[16] * sa;
    col[lane] = silu_f(raw[9 * p * 64 + 32 * ho + chl] * sa + sb);
  }
  if (wid == 5) {
    wait_tags<1>(ws, uprod, W_U2, lane < 32 && lane != uidx, ep);
    lds_flag_set(lds, 3, ep);
  } else {
    lds_flag_wait(lds, 3, ep);
  }
  asm volatile("" : "+v"(ve[0]), "+v"(ve[1]), "+v"(vo[0]), "+v"(vo[1]));
  ld_u(L, 0); ld_u(L, 1); ld_u(L, 2);
  const __amdgpu_buffer_rsrc_t rv = __builtin_amdgcn_make_buffer_rsrc((void*)(ws + OFF_V2), (short)0, (int)V_BYTES, 0x00020000);
  f32x4 hl[2], hr[2];
  u32x4 o[4];
  halo_ops(lds, col, T.tx, kg, hl, hr);
  row_transform(ve, vo, hl, hr, af, o);
  if (!have_v) { o[0] = o[1] = o[2] = o[3] = u32x4{0u, 0u, 0u, 0u}; }
  put_row(lds, rv, T, ho, 9 * p, false, i16, kg, o);
  wait_vm<0>();
}

constexpr int xpiece(int k) { return k < 24 ? LDS_U + 2 * USLOT + k * 1024 : LDS_U + (k - 24) * 1024; }

DI void epi2_mfma(const Comp& s, char* lds, char* ws, const Tile& T, u16* __restrict__ out, int wid, int lane) {
  const int P = wid & 1;
  constexpr int MODE = 2; (void)MODE; (void)ws;
  const int i16 = lane & 15, kg = lane >> 4, ho = wid >> 1;
  bar();
  f32x4 ye[4][2], yo[4][2];
  if (P == 0) exchange<0>(s, lds, wid, lane, ye, yo); else exchange<1>(s, lds, wid, lane, ye, yo);
  if (wid == 0) { STAMP(12); }
  const size_t off0 = ((size_t)(T.b * NCH + T.nt * 64 + 32 * ho + i16)) * HW + (T.y0 + 4 * P) * 64 + 32 * T.tx + 8 * kg;
  [[maybe_unused]] const __amdgpu_buffer_rsrc_t ro = __builtin_amdgcn_make_buffer_rsrc((void*)out, (short)0, 2 * NCH * HW * 2, 0x00020000);
#pragma unroll
  for (int f = 0; f < 2; ++f)
#pragma unroll
    for (int r = 0; r < 4; ++r) {
      const int k = (ho * 2 + f) * 8 + 4 * P + r;
      const u32x4 xv = *(const u32x4*)(lds + (k < 24 ? LDS_U + 2 * USLOT + k * 1024 : LDS_U + (k - 24) * 1024) + lane * 16);
      u32x4 o;
#pragma unroll
      for (int j = 0; j < 4; ++j) o[j] = pack_bf16(ye[r][f][j] + bf_lo(xv[j]), yo[r][f][j] + bf_hi(xv[j]));
      __builtin_amdgcn_raw_buffer_store_b128(o, ro, (int)((off0 + (size_t)f * 16 * HW + r * 64) * 2), 0, OUT_AUX);
    }
}

template <int MODE> DI void loader_loop(const Loader& L, int uidx, int uprod, u32x4 (&ep8)[8], char* lds, char* ws, const Tile& T, unsigned ep, int w, int lane) {
  const int pty = T.ty - 1 + (lane >> 3);
  const bool need = lane < 24 && pty >= 0 && pty < 8 && (lane & 7) != T.nt;
  const int pbid = need ? tile_bid(T.b, pty, T.tx, lane & 7) : T.bid;
  constexpr int WV = MODE == 1 ? W_V1 : W_V2;
  lds_bar();
  if (w == 0) { STAMPR(20); }
  u32x4 tg = {0u, 0u, 0u, 0u};
  if (w == 0) {
    const char* tp = ws + OFF_LINES + pbid * 128 + WV * 4;
    asm volatile("global_load_dwordx4 %0, %1, off sc1" : "=&v"(tg) : "v"(tp) : "memory");
  }
  bar();
  ld_u(L, 3);
  if constexpr (MODE == 1) {
    if (w == 1) { if (!lds_flag_test(lds, 12, ep)) { wait_tags<1>(ws, uprod, W_U1, lane < 32 && lane != uidx, ep); lds_flag_set(lds, 12, ep); } }
    else lds_flag_wait(lds, 12, ep);
  }
  if (MODE == 1 && w == 3) {
    const __amdgpu_buffer_rsrc_t re = __builtin_amdgcn_make_buffer_rsrc((void*)(ws + OFF_EMBP), (short)0, 8 * 32 * 512, 0x00020000);
#pragma unroll
    for (int i = 0; i < 8; ++i) ep8[i] = __builtin_amdgcn_raw_buffer_load_b128(re, (T.nt * 32 + (lane >> 4) * 8 + i) * 512 + T.b * 256 + (lane & 15) * 16, 0, 16);
  }
  bar();
  ld_u(L, 4);
  bool early = false;
  if (w == 0) {
    wait_vm<12>();
    asm volatile("" : "+v"(tg) :: "memory");
    early = __all(!need || (tg[0] == ep && tg[1] == ep && tg[2] == ep && tg[3] == ep));
    if (early) lds_flag_set(lds, MODE, ep);
  }
  wait_vm<6>();
  bar();
  if (w == 0) {
    STAMPR(22);
    if (!early) { wait_tags<1>(ws, pbid, WV, need, ep); lds_flag_set(lds, MODE, ep); }
    STAMPR(23);
  } else {
    lds_flag_wait(lds, MODE, ep);
  }
  ld_v(L, 4); ld_v(L, 5); ld_u(L, 5);
  wait_vm<11>();
  bar();
#pragma unroll 1
  for (int t = 4; t < NUNIT - 2; ++t) {
    ld_v(L, t + 2); ld_u(L, t + 2);
    wait_vm<11>();
    bar();
  }
}

template <int MODE> DI void mfma_loop(Comp& s, char* ws, const u16* __restrict__ w2, const Tile& T, unsigned ldsb, unsigned ep, int wid, int lane) {
  const int bid = T.bid;
  lds_bar();
  lds_rd<0>(s.B[0][0], s.bbase); lds_rd<1024>(s.B[0][1], s.bbase); lds_rd<0>(s.A[0], s.abase);
  lds_rd<4096>(s.B[0][2], s.bbase); lds_rd<5120>(s.B[0][3], s.bbase); lds_rd<8192>(s.B[0][4], s.bbase); lds_rd<9216>(s.B[0][5], s.bbase);
  lds_rd<1024>(s.A[1], s.abase); lds_rd<2048>(s.A[2], s.abase); lds_rd<3072>(s.A[3], s.abase);
  wait_lgkm<3>();
  s.anxt = s.abase;
  unsigned voff = 0, uoff = 0;
  unit<MODE, 0, true>(s, voff, uoff);
  wait_vm<0>();
  set_tag(ws, bid, (MODE == 1 ? W_V1 : W_V2) + wid, ep, fresh_lane());
  if (MODE == 1) u_touch(ldsb, w2, 1, T, (T.b * 8 + T.ty) * 2 + T.tx, wid, fresh_lane());
  unit<MODE, 1, true>(s, voff, uoff);
#pragma unroll 1
  for (int c = 1; c < 16; ++c) {
    unit<MODE, 0>(s, voff, uoff);
    unit<MODE, 1>(s, voff, uoff);
  }
  asm volatile("s_waitcnt lgkmcnt(0)\n\ts_nop 15" ::: "memory");
}
__global__ __launch_bounds__(512, 2) void k_main(const u16* __restrict__ x, const u16* __restrict__ temb, const u16* __restrict__ g1, const u16* __restrict__ be1,
                                                 const u16* __restrict__ w1, const u16* __restrict__ wemb, const u16* __restrict__ g2,
                                                 const u16* __restrict__ be2, const u16* __restrict__ w2, u16* __restrict__ out, char* __restrict__ ws) {
  __shared__ __attribute__((aligned(16))) char lds[LDS_TOTAL];
  const int tid = threadIdx.x, lane = tid & 63;
  const int wid = __builtin_amdgcn_readfirstlane(tid >> 6);
  const int bid = blockIdx.x, xcd = bid & 7, slot = bid >> 3;
  Tile T;
  T.bid = bid; T.b = xcd >> 2; T.nt = (xcd & 1) * 4 + (slot & 3); T.ty = ((xcd >> 1) & 1) * 4 + (slot >> 3); T.tx = (slot >> 2) & 1; T.y0 = T.ty * 8;
  const unsigned ldsb = (unsigned)(size_t)lds;
  const int p = wid & 1, ho = (wid >> 1) & 1;
  {
    const __amdgpu_buffer_rsrc_t rc = __builtin_amdgcn_make_buffer_rsrc((void*)__builtin_amdgcn_s_getpc(), (short)0, CODE_BYTES, 0x00020000);
    asm volatile("s_mov_b32 m0, %0\n\ts_nop 0\n\tbuffer_load_dword %1, %2, 0 offen lds" :: "s"(ldsb + M_DUMP), "v"((unsigned)tid * 128u), "s"(rc) : "memory");
  }
  const unsigned ep = __builtin_amdgcn_readfirstlane(__hip_atomic_load((gu32*)(ws + OFF_EPOCH), RLX_AGENT)) + 1u;
  float* red = (float*)(lds + M_RED);

  if (wid >= 4) {
    const int w = wid - 4;
    const int uidx = (T.b * 8 + T.ty) * 2 + T.tx;
    const int rho[1] = {9 * p};
    const char* stage = lds + LDS_U + w * 9216;
    XRows<1> X;
    rows_load<1>(X, rho, ldsb, x, g1, be1, T, ho, lane);
    u32x4 we[3];
    unsigned short te[2][3];
    if (w == 3) {
#pragma unroll
      for (int i = 0; i < 3; ++i) {
        const int k = 24 * uidx + 3 * (lane >> 3) + i;
        we[i] = *(const u32x4*)(wemb + (size_t)k * 512 + T.nt * 64 + 8 * (lane & 7));
        te[0][i] = temb[k]; te[1][i] = temb[NEMB + k];
      }
    }
    wait_vm<0>();
    u_stage(ldsb + LDS_U + w * 9216, w1, 0, T, uidx, w, lane);
    rows_fetch<1>(X, rho, 9 * p, 9 * p, lds, T, ho, lane);
    wait_vm<0>();
    { constexpr int MODE = 1; (void)MODE; if (w == 1) { STAMPR(48); } }
    {
      u32x4 dw1[9];
      u_fetch(dw1, stage, lane);
      u_emit(dw1, ws, 0, T, uidx, w, lane);
    }
    { constexpr int MODE = 1; (void)MODE; if (w == 1) { STAMPR(50); } }
    if (w == 3) {
      float a[2][8];
#pragma unroll
      for (int j = 0; j < 8; ++j) a[0][j] = a[1][j] = 0.f;
#pragma unroll
      for (int i = 0; i < 3; ++i) {
        const float t0 = silu_f(__uint_as_float((unsigned)te[0][i] << 16)), t1 = silu_f(__uint_as_float((unsigned)te[1][i] << 16));
#pragma unroll
        for (int j = 0; j < 8; ++j) {
          const float wv = (j & 1) ? bf_hi(we[i][j >> 1]) : bf_lo(we[i][j >> 1]);
          a[0][j] += t0 * wv; a[1][j] += t1 * wv;
        }
      }
      float* sc = (float*)(lds + M_COL);
#pragma unroll
      for (int j = 0; j < 8; ++j) { sc[(lane >> 3) * 128 + (lane & 7) * 8 + j] = a[0][j]; sc[(lane >> 3) * 128 + 64 + (lane & 7) * 8 + j] = a[1][j]; }
      float e0 = 0.f, e1 = 0.f;
#pragma unroll
      for (int g = 0; g < 8; ++g) { e0 += sc[g * 128 + lane]; e1 += sc[g * 128 + 64 + lane]; }
      const __amdgpu_buffer_rsrc_t re = __builtin_amdgcn_make_buffer_rsrc((void*)(ws + OFF_EMBP), (short)0, 8 * 32 * 512, 0x00020000);
      __builtin_amdgcn_raw_buffer_store_b32(__float_as_uint(e0), re, (T.nt * 32 + uidx) * 512 + lane * 4, 0, 16);
      __builtin_amdgcn_raw_buffer_store_b32(__float_as_uint(e1), re, (T.nt * 32 + uidx) * 512 + 256 + lane * 4, 0, 16);
    }
    wait_vm<0>();
    set_tag(ws, T.bid, W_U1 + w, ep, lane);
    { constexpr int MODE = 1; (void)MODE; if (w == 1) { STAMPR(52); } }
    lds_bar();
    Loader L;
    L.nt = T.nt;
    L.rv = __builtin_amdgcn_make_buffer_rsrc((void*)(ws + OFF_V1), (short)0, (int)V_BYTES, 0x00020000);
    L.ru = __builtin_amdgcn_make_buffer_rsrc((void*)(ws + OFF_U1), (short)0, (int)U_BYTES, 0x00020000);
    L.vmid = lane * 16;
    L.vfirst = (T.y0 == 0 && !(w & 1)) ? 0x80000000u : L.vmid;
    L.vlast = (T.y0 == 56 && (w & 1)) ? 0x80000000u : L.vmid;
    L.sv = (unsigned)T.b * 16u * 524288u + (unsigned)(w >> 1) * 131072u + (unsigned)((T.y0 + 5 * (w & 1)) * 2048 + T.tx * 1024) - 2048u;
    L.su = (unsigned)T.nt * 786432u + (unsigned)w * 6144u;
    L.mv = ldsb + (w >> 1) * 10240 + (w & 1) * 5120;
    L.mu = ldsb + LDS_U + w * 6144;
    const int uprod = tile_bid((lane >> 4) & 1, (lane >> 1) & 7, lane & 1, T.nt);
    u32x4 tg = {0u, 0u, 0u, 0u};
    if (w == 1) {
      const char* tp = ws + OFF_LINES + uprod * 128 + W_U1 * 4;
      asm volatile("global_load_dwordx4 %0, %1, off sc1" : "=&v"(tg) : "v"(tp) : "memory");
    }
    rows_column<1>(X, lds, wid, lane);
    rows_emit<1>(X, rho, 0, 1, lds, ws, T, wid, ho, lane);
    if (w == 1) {
      wait_vm<0>();
      asm volatile("" : "+v"(tg) :: "memory");
      const bool ok = tg[0] == ep && tg[1] == ep && tg[2] == ep && tg[3] == ep;
      const bool need = lane < 32 && lane != uidx, own = need && (lane >> 2) == T.nt;
      if (!__all(!own || ok)) wait_tags<1>(ws, uprod, W_U1, own, ep);
      if (__all(!need || ok)) lds_flag_set(lds, 12, ep);
      lds_flag_set(lds, 0, ep);
    } else {
      lds_flag_wait(lds, 0, ep);
    }
    { constexpr int MODE = 1; (void)MODE; if (w == 1) { STAMPR(53); } }
    ld_u(L, 0); ld_u(L, 1); ld_u(L, 2);
    wait_vm<0>();
    u32x4 ep8[8];
    loader_loop<1>(L, uidx, uprod, ep8, lds, ws, T, ep, w, lane);
    if (w == 3) {
      f32x4 e = {0.f, 0.f, 0.f, 0.f};
#pragma unroll
      for (int i = 0; i < 8; ++i) e += __builtin_bit_cast(f32x4, ep8[i]);
#pragma unroll
      for (int j = 0; j < 4; ++j) { e[j] += __shfl_xor(e[j], 16); e[j] += __shfl_xor(e[j], 32); }
      if (lane < 16) *(f32x4*)(lds + M_EMB + lane * 16) = e;
    }
    wait_vm<0>();
    bar();
    bar();
    L.rv = __builtin_amdgcn_make_buffer_rsrc((void*)(ws + OFF_V2), (short)0, (int)V_BYTES, 0x00020000);
    L.ru = __builtin_amdgcn_make_buffer_rsrc((void*)(ws + OFF_U2), (short)0, (int)U_BYTES, 0x00020000);
    epi1_loader(L, w2, uprod, uidx, lds, ws, T, ep, wid, lane);
    loader_loop<2>(L, uidx, uprod, ep8, lds, ws, T, ep, w, lane);
    {
      const __amdgpu_buffer_rsrc_t rx = __builtin_amdgcn_make_buffer_rsrc((void*)x, (short)0, 2 * NCH * HW * 2, 0x00020000);
      const unsigned vx = (lane & 15) * (HW * 2) + (lane >> 4) * 16;
      const unsigned sx = (unsigned)((T.b * NCH + T.nt * 64) * HW + T.y0 * 64 + 32 * T.tx) * 2u;
#pragma unroll
      for (int q = 0; q < 6; ++q) { const int k = w * 6 + q; dma1(ldsb + xpiece(0) + k * 1024, vx, rx, sx + (unsigned)(k >> 3) * 16u * HW * 2u + (unsigned)(k & 7) * 128u); }
      wait_vm<6>();
      bar();
#pragma unroll
      for (int q = 0; q < 2; ++q) { const int k = 24 + w * 2 + q; dma1(ldsb + xpiece(24) + (k - 24) * 1024, vx, rx, sx + (unsigned)(k >> 3) * 16u * HW * 2u + (unsigned)(k & 7) * 128u); }
      bar();
      wait_vm<0>();
      bar();
    }
    return;
  }

  const int i16 = lane & 15, kg = lane >> 4;
  {
    constexpr int MODE = 1; (void)MODE;
    if (wid == 0) { STAMP(0); STAMPR(4); }
    const int rho[4] = {4 * p + 1, 4 * p + 2, 4 * p + 3, 4 * p + 4};
    XRows<4> X;
    rows_load<4>(X, rho, ldsb, x, g1, be1, T, ho, lane);
    unsigned short g2c = 0, b2c = 0;
    if (wid == 1) { g2c = g2[T.nt * 64 + lane]; b2c = be2[T.nt * 64 + lane]; }
    wait_vm<0>();
    rows_fetch<4>(X, rho, 4 * p + 1 + (lane >> 5), 4 * p + 3 + (lane >> 5), lds, T, ho, lane);
    rows_stats<4, 4>(X, red, wid, lane);
    if (wid == 1) {
      if (lane < 8) ((float*)(lds + M_ZERO))[lane] = 0.f;
      ((float*)(lds + M_GAM))[lane] = __uint_as_float((unsigned)g2c << 16);
      ((float*)(lds + M_BET))[lane] = __uint_as_float((unsigned)b2c << 16);
    }
    asm volatile("s_waitcnt lgkmcnt(0)" ::: "memory");
    lds_flag_set(lds, 4 + wid, ep);
    if (wid == 0) {
      STAMP(24);
#pragma unroll
      for (int k = 1; k < 4; ++k) lds_flag_wait(lds, 4 + k, ep);
      float tot, m2;
      block_stats(red, tot, m2);
      group_stats(ws, 0, T, tot, m2, ep, lane);
      group_stats_wait(ws, 0, T, tot, m2, ep, red, lane);
      STAMPR(54);
    }
    lds_bar();
    if (wid == 0) { STAMP(25); }
    rows_column<4>(X, lds, wid, lane);
    rows_emit<4>(X, rho, 0, 4, lds, ws, T, wid, ho, lane);
  }
  Comp s;
  s.abase = ldsb + p * 10240 + i16 * 64 + vpos(kg, i16) * 16;
  s.bbase = ldsb + LDS_U + p * 12288 + ho * 2048 + lane * 16;
  {
    constexpr int MODE = 1; (void)MODE;
    if (wid == 0) { STAMP(1); }
    mfma_loop<1>(s, ws, w2, T, ldsb, ep, wid, lane);
    if (wid == 0) { STAMP(2); STAMPR(5); }
    bar();
    epi1_mfma(s, lds, ws, T, ep, wid, fresh_lane());
  }
  {
    constexpr int MODE = 2; (void)MODE;
    if (wid == 0) { STAMP(0); STAMPR(4); }
      if (wid == 0) { STAMP(1); }
    mfma_loop<2>(s, ws, w2, T, ldsb, ep, wid, lane);
    if (wid == 0) { STAMP(2); STAMPR(5); }
    epi2_mfma(s, lds, ws, T, out, wid, fresh_lane());
  }
  const int le = fresh_lane();
  if (wid == 0 && le < 16) ((unsigned*)(lds + M_RED + 96))[le] = 0u;
  if (bid == 0 && wid == 0 && le == 0) __hip_atomic_store((gu32*)(ws + OFF_EPOCH), ep, RLX_AGENT);
}

extern "C" void kernel_launch(void* const* d_in, const int* in_sizes, int n_in, void* d_out, int out_size, void* d_ws, size_t ws_size,
                              hipStream_t stream) {
  k_main<<<256, 512, 0, stream>>>((const u16*)d_in[0], (const u16*)d_in[1], (const u16*)d_in[2], (const u16*)d_in[3], (const u16*)d_in[4],
                                  (const u16*)d_in[5], (const u16*)d_in[6], (const u16*)d_in[7], (const u16*)d_in[8], (u16*)d_out, (char*)d_ws);
}
```

```cpp
#include <hip/hip_runtime.h>
#include <stdint.h>
#include <utility>

#define DI __device__ __forceinline__
typedef __bf16 bf16x8 __attribute__((ext_vector_type(8)));
typedef __bf16 bf16x2 __attribute__((ext_vector_type(2)));
typedef float f32x4 __attribute__((ext_vector_type(4)));
typedef float f32x2 __attribute__((ext_vector_type(2)));
typedef unsigned u32x4 __attribute__((ext_vector_type(4)));
typedef unsigned short u16;
#define GAS __attribute__((address_space(1)))
typedef GAS unsigned long long gu64;
typedef GAS unsigned gu32;
#define RLX_AGENT __ATOMIC_RELAXED, __HIP_MEMORY_SCOPE_AGENT

constexpr int NCH = 512, HW = 4096, NEMB = 768;
constexpr float GN_EPS = 1e-5f;
constexpr unsigned SPIN_CAP = 1u << 20;
constexpr int CODE_BYTES = 48 * 1024;

constexpr size_t U_BYTES = (size_t)8 * 16 * 49152;
constexpr size_t V_BYTES = (size_t)2 * 16 * 4 * 131072;
constexpr size_t OFF_U1 = 0, OFF_U2 = U_BYTES;
constexpr size_t OFF_V1 = 2 * U_BYTES, OFF_V2 = OFF_V1 + V_BYTES;
constexpr size_t OFF_EPOCH = OFF_V2 + V_BYTES;
constexpr size_t OFF_GRAN = OFF_EPOCH + 256;
constexpr size_t OFF_LINES = OFF_GRAN + 8192;
constexpr int W_HALO = 0, W_V1 = 4, W_V2 = 8, W_U1 = 12, W_U2 = 16;
constexpr size_t OFF_EMBP = OFF_LINES + 256 * 128;
constexpr size_t OFF_HALO = OFF_EMBP + 8 * 32 * 512;
constexpr int HALO_STRIDE = 18432;

DI float bf_lo(unsigned v) { return __uint_as_float(v << 16); }
DI float bf_hi(unsigned v) { return __uint_as_float(v & 0xffff0000u); }
DI unsigned pack_bf16(float lo, float hi) {
  f32x2 v = {lo, hi};
  bf16x2 b = __builtin_convertvector(v, bf16x2);
  return __builtin_bit_cast(unsigned, b);
}
DI float silu_f(float y) { return y * __builtin_amdgcn_rcpf(1.f + __builtin_amdgcn_exp2f(y * -1.44269504088896341f)); }
DI f32x2 pk_sub(f32x2 a, f32x2 b) { return __builtin_elementwise_fma(b, f32x2{-1.f, -1.f}, a); }
DI f32x2 lo2(const f32x4& v) { return __builtin_shufflevector(v, v, 0, 1); }
DI f32x2 hi2(const f32x4& v) { return __builtin_shufflevector(v, v, 2, 3); }
DI f32x4 cat2(f32x2 a, f32x2 b) { return __builtin_shufflevector(a, b, 0, 1, 2, 3); }
DI f32x4 sub4(const f32x4& a, const f32x4& b) { return cat2(pk_sub(lo2(a), lo2(b)), pk_sub(hi2(a), hi2(b))); }
DI f32x2 silu_aff2(f32x2 h, f32x2 sa, f32x2 sb) {
  const f32x2 y = __builtin_elementwise_fma(h, sa, sb), t = y * -1.44269504088896341f;
  const f32x2 d = f32x2{__builtin_amdgcn_exp2f(t[0]), __builtin_amdgcn_exp2f(t[1])} + 1.f;
  return y * f32x2{__builtin_amdgcn_rcpf(d[0]), __builtin_amdgcn_rcpf(d[1])};
}
template <int CTRL, int RM = 0xf> DI float dpp0(float v) {
  return __builtin_bit_cast(float, __builtin_amdgcn_update_dpp(0, __builtin_bit_cast(int, v), CTRL, RM, 0xf, true));
}
DI float dpp_prev(float edge, float v) {
  return __builtin_bit_cast(float, __builtin_amdgcn_update_dpp(__builtin_bit_cast(int, edge), __builtin_bit_cast(int, v), 0x111  , 0xf, 0xf, false));
}
DI float dpp_next(float edge, float v) {
  return __builtin_bit_cast(float, __builtin_amdgcn_update_dpp(__builtin_bit_cast(int, edge), __builtin_bit_cast(int, v), 0x101  , 0xf, 0xf, false));
}
DI float wave_sum(float v) {
  v += dpp0<0x111>(v); v += dpp0<0x112>(v); v += dpp0<0x114>(v); v += dpp0<0x118>(v);
  v += dpp0<0x142, 0xa>(v);
  v += dpp0<0x143, 0xc>(v);
  return __builtin_bit_cast(float, __builtin_amdgcn_readlane(__builtin_bit_cast(int, v), 63));
}
DI int vpos(int o, int xt) { return o ^ ((xt >> 1) & 3); }
DI void bar() { asm volatile("s_barrier" ::: "memory"); }
DI int fresh_lane() { int l; asm volatile("v_mbcnt_lo_u32_b32 %0, -1, 0\n\tv_mbcnt_hi_u32_b32 %0, -1, %0" : "=v"(l)); return l; }
DI void lds_bar() { asm volatile("s_waitcnt lgkmcnt(0)\n\ts_barrier" ::: "memory"); }
template <int N> DI void wait_vm() { asm volatile("s_waitcnt vmcnt(%0)" :: "i"(N) : "memory"); }

DI void gn_combine(float s, float q, bool on, float n, float& mean, float& rstd) {
  const float tot = wave_sum(on ? s : 0.f);
  mean = tot * (1.f / (32.f * n));
  const float d = s * (1.f / n) - mean;
  const float m2 = wave_sum(on ? (q + n * d * d) : 0.f);
  rstd = __builtin_amdgcn_rsqf(m2 * (1.f / (32.f * n)) + GN_EPS);
}

constexpr int VSLOT = 20480, NVSLOT = 4;
constexpr int USLOT = 24576, NUSLOT = 3;
constexpr int LDS_U = NVSLOT * VSLOT, LDS_MISC = LDS_U + NUSLOT * USLOT, LDS_TOTAL = 163840;
constexpr int NUNIT = 32;
constexpr int M_EMB = LDS_MISC, M_GAM = LDS_MISC + 256, M_BET = LDS_MISC + 512, M_RED = LDS_MISC + 768, M_ZERO = LDS_MISC + 960, M_RAW = LDS_MISC + 1024, M_COL = LDS_MISC + 3584, M_DUMP = LDS_MISC + 7680;

struct Tile { int bid, b, nt, ty, tx, y0; };
DI int tile_bid(int b, int ty, int tx, int nt) { return (((ty & 3) * 8 + tx * 4 + (nt & 3)) << 3) | (b * 4 + (ty >> 2) * 2 + (nt >> 2)); }
DI int chunk_of(int j, int nt) { return j < 2 ? 2 * nt + j : (j - 2 < 2 * nt ? j - 2 : j); }

struct Loader {
  __amdgpu_buffer_rsrc_t rv, ru;
  unsigned vfirst, vmid, vlast;
  unsigned sv, su;
  unsigned mv, mu;
  int nt;
};
DI void dma1(unsigned m0, unsigned voff, __amdgpu_buffer_rsrc_t r, unsigned soff) {
  asm volatile("s_mov_b32 m0, %0\n\ts_nop 0\n\tbuffer_load_dwordx4 %1, %2, %3 offen lds" :: "s"(m0), "v"(voff), "s"(r), "s"(soff) : "memory");
}
DI void dma1_dev(unsigned m0, unsigned voff, __amdgpu_buffer_rsrc_t r, unsigned soff) {
  asm volatile("s_mov_b32 m0, %0\n\ts_nop 0\n\tbuffer_load_dwordx4 %1, %2, %3 offen sc1 lds" :: "s"(m0), "v"(voff), "s"(r), "s"(soff) : "memory");
}
DI void ld_u(const Loader& L, int k) {
  const unsigned mu = L.mu + (unsigned)(k % NUSLOT) * USLOT, su = L.su + (unsigned)(chunk_of(k >> 1, L.nt) * 2 + (k & 1)) * USLOT;
  asm volatile("s_mov_b32 m0, %0\n\ts_nop 0\n\t"
               "buffer_load_dwordx4 %1, %2, %3 offen sc1 lds\n\t"
               "buffer_load_dwordx4 %1, %2, %3 offen offset:1024 sc1 lds\n\t"
               "buffer_load_dwordx4 %1, %2, %3 offen offset:2048 sc1 lds\n\t"
               "buffer_load_dwordx4 %1, %2, %3 offen offset:3072 sc1 lds"
               :: "s"(mu), "v"(L.vmid), "s"(L.ru), "s"(su) : "memory");
  asm volatile("s_mov_b32 m0, %0\n\ts_nop 0\n\t"
               "buffer_load_dwordx4 %1, %2, %3 offen sc1 lds\n\t"
               "buffer_load_dwordx4 %1, %2, %3 offen offset:1024 sc1 lds"
               :: "s"(mu + 4096), "v"(L.vmid), "s"(L.ru), "s"(su + 4096) : "memory");
}
DI void ld_v(const Loader& L, int k) {
  const unsigned mv = L.mv + (unsigned)(k & 3) * VSLOT, sv = L.sv + (unsigned)(chunk_of(k >> 1, L.nt) * 2 + (k & 1)) * 262144u;
  dma1_dev(mv, L.vfirst, L.rv, sv);
  dma1_dev(mv + 1024, L.vmid, L.rv, sv + 2048);
  dma1_dev(mv + 2048, L.vmid, L.rv, sv + 4096);
  dma1_dev(mv + 3072, L.vmid, L.rv, sv + 6144);
  dma1_dev(mv + 4096, L.vlast, L.rv, sv + 8192);
}

struct Comp {
  f32x4 acc[2][8][2];
  bf16x8 A[10];
  bf16x8 B[2][6];
  unsigned abase, bbase;
  unsigned acur, anxt, bnxt;
};
struct MD { int r, dy, o; };
constexpr MD mdecode(int m) {
  int n = 0;
  for (int r = 0; r < 10; ++r)
    for (int dy = 0; dy < 3; ++dy) {
      if (r - dy < 0 || r - dy > 7) continue;
      for (int o = 0; o < 2; ++o) { if (n == m) return MD{r, dy, o}; ++n; }
    }
  return MD{-1, -1, -1};
}
template <int OFF> DI void lds_rd(bf16x8& dst, unsigned addr) { asm volatile("ds_read_b128 %0, %1 offset:%2" : "=v"(dst) : "v"(addr), "i"(OFF)); }
template <int N> DI void wait_lgkm() { asm volatile("s_waitcnt lgkmcnt(%0)" :: "i"(N) : "memory"); }

constexpr int first_wait(int m) {
  switch (m) { case 2: return 3; case 6: return 3; case 12: return 4; case 18: return 5; case 24: return 6; case 30: return 7;
               case 36: return 8; case 42: return 9; case 46: return 10; default: return -1; }
}
template <int MODE, int E, bool FIRST, int M> DI void mstep(Comp& s) {
  constexpr MD d = mdecode(M);
  if constexpr (first_wait(M) >= 0) wait_lgkm<first_wait(M)>();
  if constexpr (FIRST && d.dy == 0) {
    if constexpr (MODE == 1) asm volatile("v_mfma_f32_16x16x32_bf16 %0, %1, %2, 0" : "=&v"(s.acc[E][d.r][d.o]) : "v"(s.B[E][d.o]), "v"(s.A[d.r]));
    else                     asm volatile("v_mfma_f32_16x16x32_bf16 %0, %1, %2, 0" : "=&v"(s.acc[E][d.r][d.o]) : "v"(s.A[d.r]), "v"(s.B[E][d.o]));
  } else {
    if constexpr (MODE == 1) asm volatile("v_mfma_f32_16x16x32_bf16 %0, %1, %2, %0" : "+v"(s.acc[E][d.r - d.dy][d.o]) : "v"(s.B[E][d.dy * 2 + d.o]), "v"(s.A[d.r]));
    else                     asm volatile("v_mfma_f32_16x16x32_bf16 %0, %1, %2, %0" : "+v"(s.acc[E][d.r - d.dy][d.o]) : "v"(s.A[d.r]), "v"(s.B[E][d.dy * 2 + d.o]));
  }
  if constexpr (M % 3 == 0) {
    constexpr int j = M / 3;
    if constexpr (j < 6) lds_rd<(4 + j) * 1024>(s.A[4 + j], s.acur);
    else if constexpr (j == 6) lds_rd<0>(s.B[E ^ 1][0], s.bnxt);
    else if constexpr (j == 7) lds_rd<1024>(s.B[E ^ 1][1], s.bnxt);
    else if constexpr (j == 8) lds_rd<0>(s.A[0], s.anxt);
    else if constexpr (j == 9) lds_rd<4096>(s.B[E ^ 1][2], s.bnxt);
    else if constexpr (j == 10) lds_rd<4096 + 1024>(s.B[E ^ 1][3], s.bnxt);
    else if constexpr (j == 11) lds_rd<8192>(s.B[E ^ 1][4], s.bnxt);
    else if constexpr (j == 12) lds_rd<8192 + 1024>(s.B[E ^ 1][5], s.bnxt);
    else lds_rd<(j - 12) * 1024>(s.A[j - 12], s.anxt);
  }
}
template <int MODE, int E, bool FIRST, int... Ms> DI void msteps(Comp& s, std::integer_sequence<int, Ms...>) { (mstep<MODE, E, FIRST, Ms>(s), ...); }

template <int MODE, int E, bool FIRST = false> DI void unit(Comp& s, unsigned& voff, unsigned& uoff) {
  bar();
  s.acur = s.anxt;
  voff = (voff + VSLOT) == NVSLOT * VSLOT ? 0u : voff + VSLOT;
  s.anxt = s.abase + voff;
  uoff = (uoff + USLOT) == NUSLOT * USLOT ? 0u : uoff + USLOT;
  s.bnxt = s.bbase + uoff;
  msteps<MODE, E, FIRST>(s, std::make_integer_sequence<int, 48>{});
  wait_lgkm<3>();
}

struct Affine { f32x2 sa[4], sb[4]; };
DI void row_transform(const f32x4 (&ye)[2], const f32x4 (&yo)[2], const f32x4 (&hl)[2], const f32x4 (&hr)[2], const Affine& af, u32x4 (&o)[4]) {
#pragma unroll
  for (int q = 0; q < 4; ++q) {
    const f32x2 he = (q & 1) ? hi2(ye[q >> 1]) : lo2(ye[q >> 1]), ho2 = (q & 1) ? hi2(yo[q >> 1]) : lo2(yo[q >> 1]);
    const f32x2 l2 = (q & 1) ? hi2(hl[q >> 1]) : lo2(hl[q >> 1]), r2 = (q & 1) ? hi2(hr[q >> 1]) : lo2(hr[q >> 1]);
    const f32x2 d1 = silu_aff2(he, af.sa[q], af.sb[q]);
    const f32x2 d2 = silu_aff2(ho2, af.sa[q], af.sb[q]);
    const f32x2 d0 = {dpp_prev(l2[0], d2[0]), dpp_prev(l2[1], d2[1])}, d3 = {dpp_next(r2[0], d1[0]), dpp_next(r2[1], d1[1])};
    const f32x2 t0 = pk_sub(d0, d2), t1 = d1 + d2, t2 = pk_sub(d2, d1), t3 = pk_sub(d1, d3);
    o[0][q] = pack_bf16(t0[0], t0[1]); o[1][q] = pack_bf16(t1[0], t1[1]); o[2][q] = pack_bf16(t2[0], t2[1]); o[3][q] = pack_bf16(t3[0], t3[1]);
  }
}
DI void halo_ops(const char* lds, const float* c, int tx, int kg, f32x4 (&hl)[2], f32x4 (&hr)[2]) {
  const float* z = (const float*)(lds + M_ZERO);
  const float* pl = tx == 1 ? c + 8 * kg : z;
  const float* pr = tx == 0 ? c + 8 * kg : z;
  hl[0] = *(const f32x4*)pl; hl[1] = *(const f32x4*)(pl + 4);
  hr[0] = *(const f32x4*)pr; hr[1] = *(const f32x4*)(pr + 4);
}

DI f32x4 add4_here(const f32x4& a, const f32x4& b) {
  f32x2 lo, hi;
  asm volatile("v_pk_add_f32 %0, %1, %2" : "=v"(lo) : "v"(lo2(a)), "v"(lo2(b)));
  asm volatile("v_pk_add_f32 %0, %1, %2" : "=v"(hi) : "v"(hi2(a)), "v"(hi2(b)));
  return cat2(lo, hi);
}
template <int P> DI void exchange(const Comp& s, char* lds, int wid, int lane, f32x4 (&ye)[4][2], f32x4 (&yo)[4][2]) {
  char* xb = lds + wid * 16384 + lane * 16;
  const char* rb = lds + (wid ^ 1) * 16384 + lane * 16;
#pragma unroll
  for (int r = 0; r < 4; ++r)
#pragma unroll
    for (int o = 0; o < 2; ++o) {
      if constexpr (P == 0) {
        *(f32x4*)(xb + (r * 4 + o * 2) * 1024) = add4_here(s.acc[0][4 + r][o], s.acc[1][4 + r][o]);
        *(f32x4*)(xb + (r * 4 + o * 2 + 1) * 1024) = s.acc[1][4 + r][o];
      } else {
        *(f32x4*)(xb + (r * 4 + o * 2) * 1024) = s.acc[0][r][o];
        *(f32x4*)(xb + (r * 4 + o * 2 + 1) * 1024) = add4_here(s.acc[0][r][o], s.acc[1][r][o]);
      }
      __builtin_amdgcn_sched_barrier(0);
    }
  lds_bar();
#pragma unroll
  for (int o = 0; o < 2; ++o) {
    f32x4 r0[4], r1[4];
#pragma unroll
    for (int r = 0; r < 4; ++r) { r0[r] = *(const f32x4*)(rb + (r * 4 + o * 2) * 1024); r1[r] = *(const f32x4*)(rb + (r * 4 + o * 2 + 1) * 1024); }
#pragma unroll
    for (int r = 0; r < 4; ++r) {
      if constexpr (P == 0) { ye[r][o] = add4_here(s.acc[0][r][o], s.acc[1][r][o]) + r0[r]; yo[r][o] = sub4(s.acc[1][r][o], r1[r]); }
      else                  { ye[r][o] = r0[r] + s.acc[0][4 + r][o]; yo[r][o] = sub4(r1[r], add4_here(s.acc[0][4 + r][o], s.acc[1][4 + r][o])); }
    }
    __builtin_amdgcn_sched_barrier(0);
  }
}

DI void load_affine(Affine& af, const char* lds, int ho, int kg) {
  const float* red = (const float*)(lds + M_RED);
  const float mean = red[16], rstd = red[17];
  const f32x4 g0 = *(const f32x4*)(lds + M_GAM + (32 * ho + 8 * kg) * 4), g1 = *(const f32x4*)(lds + M_GAM + (32 * ho + 8 * kg) * 4 + 16);
  const f32x4 b0 = *(const f32x4*)(lds + M_BET + (32 * ho + 8 * kg) * 4), b1 = *(const f32x4*)(lds + M_BET + (32 * ho + 8 * kg) * 4 + 16);
#pragma unroll
  for (int q = 0; q < 4; ++q) {
    const f32x2 g = q == 0 ? lo2(g0) : q == 1 ? hi2(g0) : q == 2 ? lo2(g1) : hi2(g1);
    const f32x2 bb = q == 0 ? lo2(b0) : q == 1 ? hi2(b0) : q == 2 ? lo2(b1) : hi2(b1);
    af.sa[q] = g * rstd; af.sb[q] = bb - af.sa[q] * mean;
  }
}
DI void set_tag(char* ws, int bid, int word, unsigned ep, int lane) {
  if (lane == 0) __hip_atomic_store((gu32*)(ws + OFF_LINES) + bid * 32 + word, ep, RLX_AGENT);
}
template <int NQ> DI void wait_tags(char* ws, int who, int w0, bool need, unsigned ep) {
  const __amdgpu_buffer_rsrc_t rl = __builtin_amdgcn_make_buffer_rsrc((void*)(ws + OFF_LINES), (short)0, 256 * 128, 0x00020000);
  for (unsigned spins = 0;; ++spins) {
    bool ok = true;
#pragma unroll
    for (int q = 0; q < NQ; ++q) {
      const u32x4 v = __builtin_amdgcn_raw_buffer_load_b128(rl, who * 128 + w0 * 4 + q * 16, 0, 16);
      ok = ok && v[0] == ep && v[1] == ep && v[2] == ep && v[3] == ep;
    }
    if (__all(!need || ok)) break;
    if (spins > SPIN_CAP) break;
    __builtin_amdgcn_s_sleep(2);
    asm volatile("" ::: "memory");
  }
  asm volatile("" ::: "memory");
}
DI unsigned lds_key(unsigned ep, int idx) { return (ep * 16u + (unsigned)idx) * 0x9E3779B1u + 0x3C6EF372u; }
DI void lds_flag_set(char* lds, int idx, unsigned ep) {
  const unsigned key = lds_key(ep, idx);
  __hip_atomic_store((__attribute__((address_space(3))) unsigned*)(lds + M_RED + 96 + idx * 4), key, __ATOMIC_RELAXED, __HIP_MEMORY_SCOPE_WORKGROUP);
}
DI void lds_flag_wait(char* lds, int idx, unsigned ep) {
  const unsigned key = lds_key(ep, idx);
  for (unsigned spins = 0; spins < (SPIN_CAP << 4); ++spins) {
    const unsigned v = __hip_atomic_load((__attribute__((address_space(3))) unsigned*)(lds + M_RED + 96 + idx * 4), __ATOMIC_RELAXED, __HIP_MEMORY_SCOPE_WORKGROUP);
    if (__builtin_amdgcn_readfirstlane(v) == key) break;
    __builtin_amdgcn_s_sleep(1);
  }
  asm volatile("" ::: "memory");
}
DI bool lds_flag_test(char* lds, int idx, unsigned ep) {
  const unsigned v = __hip_atomic_load((__attribute__((address_space(3))) unsigned*)(lds + M_RED + 96 + idx * 4), __ATOMIC_RELAXED, __HIP_MEMORY_SCOPE_WORKGROUP);
  return __builtin_amdgcn_readfirstlane(v) == lds_key(ep, idx);
}
DI void block_stats(const float* red, float& tot, float& m2) {
  const float s0 = red[0], s1 = red[1], s2 = red[2], s3 = red[3];
  tot = (s0 + s1) + (s2 + s3);
  const float bm = tot * (1.f / 16384.f);
  const float d0 = s0 * (1.f / 4096.f) - bm, d1 = s1 * (1.f / 4096.f) - bm, d2 = s2 * (1.f / 4096.f) - bm, d3 = s3 * (1.f / 4096.f) - bm;
  m2 = (red[8] + red[9]) + (red[10] + red[11]) + 4096.f * ((d0 * d0 + d1 * d1) + (d2 * d2 + d3 * d3));
}
DI int gran_slot(const Tile& T) { return T.ty * 4 + T.tx * 2 + (T.nt & 1); }
DI void group_stats(char* ws, int conv, const Tile& T, float tot, float m2, unsigned ep, int lane) {
  gu64* gran = (gu64*)(ws + OFF_GRAN) + (conv * 8 + T.b * 4 + (T.nt >> 1)) * 64;
  if (lane < 2) {
    const float val = lane == 0 ? tot : m2;
    __hip_atomic_store(gran + gran_slot(T) * 2 + lane, ((unsigned long long)ep << 32) | (unsigned long long)__float_as_uint(val), RLX_AGENT);
  }
}
DI void group_stats_wait(char* ws, int conv, const Tile& T, float tot, float m2, unsigned ep, float* red, int lane) {
  gu64* gran = (gu64*)(ws + OFF_GRAN) + (conv * 8 + T.b * 4 + (T.nt >> 1)) * 64;
  const bool own = (lane >> 1) == gran_slot(T);
  unsigned long long g;
  for (unsigned spins = 0;; ++spins) {
    g = __hip_atomic_load(gran + lane, RLX_AGENT);
    if (__all(own || (unsigned)(g >> 32) == ep)) break;
    if (spins > SPIN_CAP) break;
    __builtin_amdgcn_s_sleep(1);
  }
  const float val = own ? ((lane & 1) ? m2 : tot) : __uint_as_float((unsigned)g);
  const float qk = dpp0<0x101>(val);
  float mean, rstd;
  gn_combine(val, qk, !(lane & 1), 16384.f, mean, rstd);
  if (lane == 0) { red[16] = mean; red[17] = rstd; }
}

DI void put_row(char* lds, __amdgpu_buffer_rsrc_t rv, const Tile& T, int ho, int rho, bool publish, int i16, int kg, const u32x4 (&o)[4]) {
  const int pos = i16 * 64 + vpos(kg, i16) * 16;
  char* d = lds + 2 * ho * VSLOT + rho * 1024 + pos;
#pragma unroll
  for (int xi = 0; xi < 4; ++xi) *(u32x4*)(d + (xi & 1) * VSLOT + (xi >> 1) * 10240) = o[xi];
  if (publish) {
    const int so = (T.b * 16 + 2 * T.nt + ho) * 524288 + (T.y0 - 1 + rho) * 2048 + T.tx * 1024;
#pragma unroll
    for (int xi = 0; xi < 4; ++xi)
      __builtin_amdgcn_raw_buffer_store_b128(o[xi], rv, pos + so + (xi & 1) * 262144 + (xi >> 1) * 131072, 0, 16);
  }
}

template <int NR> struct XRows { unsigned v[NR][8]; unsigned short col[2]; u32x4 gv, bv; unsigned short gc, bc; };
DI int clamp_y(int y) { return min(max(y, 0), 63); }
DI void rows_stage(int rho0, int nrows, unsigned ldsb, const u16* __restrict__ x, const Tile& T, int ho, int lane) {
  const __amdgpu_buffer_rsrc_t rx = __builtin_amdgcn_make_buffer_rsrc((void*)x, (short)0, 2 * NCH * HW * 2, 0x00020000);
  const unsigned vo0 = (unsigned)((lane >> 3) * (HW * 2) + (lane & 7) * 16), vo1 = vo0 ^ 64u;
  const unsigned sb = (unsigned)((T.b * NCH + T.nt * 64 + 32 * ho) * HW * 2);
#pragma unroll 1
  for (int rho = rho0; rho < rho0 + nrows; ++rho) {
    const unsigned so = sb + (unsigned)clamp_y(T.y0 - 1 + rho) * 128u, m = ldsb + (rho * 2 + ho) * 4096;
    dma1(m, vo0, rx, so);
    dma1(m + 1024, vo1, rx, so + 8 * HW * 2);
    dma1(m + 2048, vo0, rx, so + 16 * HW * 2);
    dma1(m + 3072, vo1, rx, so + 24 * HW * 2);
  }
}
template <int NR> DI void rows_params(XRows<NR>& X, const u16* __restrict__ gam, const u16* __restrict__ bet, const Tile& T, int ho, int lane) {
  const int kg = lane >> 4, chl = lane & 31, ch = T.nt * 64 + 32 * ho;
  X.gv = *(const u32x4*)(gam + ch + 8 * kg); X.bv = *(const u32x4*)(bet + ch + 8 * kg);
  X.gc = gam[ch + chl]; X.bc = bet[ch + chl];
}
template <int NR> DI void rows_fetch(XRows<NR>& X, const int (&rho)[NR], int rc0, int rc1, const char* lds, const Tile& T, int ho, int lane) {
  const int i16 = lane & 15, kg = lane >> 4, chl = lane & 31;
  const int pos = (((T.tx * 4 + (i16 >> 2)) + 4 * (kg & 1)) & 7) * 16 + (i16 & 3) * 4;
#pragma unroll
  for (int k = 0; k < NR; ++k)
#pragma unroll
    for (int j = 0; j < 8; ++j) X.v[k][j] = *(const unsigned*)(lds + (rho[k] * 2 + ho) * 4096 + (8 * kg + j) * 128 + pos);
  const int xc = T.tx == 0 ? 32 : 31;
  const int cpos = chl * 128 + (((xc >> 3) + 4 * ((chl >> 3) & 1)) & 7) * 16 + (xc & 7) * 2;
  X.col[0] = *(const unsigned short*)(lds + (rc0 * 2 + ho) * 4096 + cpos);
  X.col[1] = *(const unsigned short*)(lds + (rc1 * 2 + ho) * 4096 + cpos);
}
template <int NR, int NI> DI void rows_stats(const XRows<NR>& X, float* red, int wid, int lane) {
  const float piv = __builtin_bit_cast(float, __builtin_amdgcn_readfirstlane(X.v[0][0] << 16));
  f32x2 sv = {0.f, 0.f}, qv = {0.f, 0.f};
#pragma unroll
  for (int k = 0; k < NI; ++k)
#pragma unroll
    for (int j = 0; j < 8; ++j) {
      const f32x2 d = f32x2{bf_lo(X.v[k][j]), bf_hi(X.v[k][j])} - piv;
      sv += d; qv = __builtin_elementwise_fma(d, d, qv);
    }
  const float s = wave_sum(sv[0] + sv[1]), q = wave_sum(qv[0] + qv[1]);
  if (lane == 0) { red[wid] = s + piv * (float)(NI * 1024); red[8 + wid] = q - s * s * (1.f / (float)(NI * 1024)); }
}
template <int NR> DI void rows_emit(const XRows<NR>& X, const int (&rho)[NR], int k0, int k1, char* lds, char* ws, const Tile& T, int wid, int ho, int lane) {
  const int i16 = lane & 15, kg = lane >> 4;
  const float* red = (const float*)(lds + M_RED);
  const float mean = red[16], rstd = red[17];
  float sa[8], sb[8];
#pragma unroll
  for (int j = 0; j < 8; ++j) {
    sa[j] = rstd * ((j & 1) ? bf_hi(X.gv[j >> 1]) : bf_lo(X.gv[j >> 1]));
    sb[j] = ((j & 1) ? bf_hi(X.bv[j >> 1]) : bf_lo(X.bv[j >> 1])) - mean * sa[j];
  }
  const float* col = (const float*)(lds + M_COL + wid * 512);
  const __amdgpu_buffer_rsrc_t rv = __builtin_amdgcn_make_buffer_rsrc((void*)(ws + OFF_V1), (short)0, (int)V_BYTES, 0x00020000);
#pragma unroll
  for (int k = 0; k < NR; ++k) {
    if (k < k0 || k >= k1) continue;
    f32x4 hl[2], hr[2];
    halo_ops(lds, col + k * 32, T.tx, kg, hl, hr);
    float t[4][8];
#pragma unroll
    for (int j = 0; j < 8; ++j) {
      const f32x2 a2 = {sa[j], sa[j]}, b2 = {sb[j], sb[j]};
      const f32x2 d = silu_aff2(f32x2{bf_lo(X.v[k][j]), bf_hi(X.v[k][j])}, a2, b2);
      t[0][j] = dpp_prev(hl[j >> 2][j & 3], d[1]) - d[1];
      t[1][j] = d[0] + d[1];
      t[2][j] = d[1] - d[0];
      t[3][j] = d[0] - dpp_next(hr[j >> 2][j & 3], d[0]);
    }
    u32x4 o[4];
#pragma unroll
    for (int xi = 0; xi < 4; ++xi)
      o[xi] = u32x4{pack_bf16(t[xi][0], t[xi][1]), pack_bf16(t[xi][2], t[xi][3]), pack_bf16(t[xi][4], t[xi][5]), pack_bf16(t[xi][6], t[xi][7])};
    const int y = T.y0 - 1 + rho[k];
    const bool halo_row = NR == 1;
    if (halo_row && (y < 0 || y > 63)) { o[0] = o[1] = o[2] = o[3] = u32x4{0u, 0u, 0u, 0u}; }
    put_row(lds, rv, T, ho, rho[k], !halo_row, i16, kg, o);
    __builtin_amdgcn_sched_barrier(0);
  }
}
template <int NR> DI void rows_column(const XRows<NR>& X, char* lds, int wid, int lane) {
  const float* red = (const float*)(lds + M_RED);
  const float mean = red[16], rstd = red[17];
  float* col = (float*)(lds + M_COL + wid * 512);
  const float sa = rstd * __uint_as_float((unsigned)X.gc << 16), sb = __uint_as_float((unsigned)X.bc << 16) - mean * sa;
  col[lane] = silu_f(__uint_as_float((unsigned)X.col[0] << 16) * sa + sb);
  if constexpr (NR > 2) col[64 + lane] = silu_f(__uint_as_float((unsigned)X.col[1] << 16) * sa + sb);
}

DI int u_row(int conv, int of, int i) { return conv == 0 ? (32 * (of >> 1) + 8 * (i >> 2) + 4 * (of & 1) + (i & 3)) : (16 * of + i); }
DI void u_load(u32x4 (&d)[9], const u16* __restrict__ w, int conv, const Tile& T, int uidx, int of, int lane) {
  const int i = lane & 15, kg = lane >> 4;
  const u16* src = w + ((size_t)(T.nt * 64 + u_row(conv, of, i)) * 512 + 32 * (uidx >> 1) + 8 * kg) * 9;
  asm volatile("global_load_dwordx4 %0, %9, off\n\tglobal_load_dwordx4 %1, %9, off offset:16\n\tglobal_load_dwordx4 %2, %9, off offset:32\n\t"
               "global_load_dwordx4 %3, %9, off offset:48\n\tglobal_load_dwordx4 %4, %9, off offset:64\n\tglobal_load_dwordx4 %5, %9, off offset:80\n\t"
               "global_load_dwordx4 %6, %9, off offset:96\n\tglobal_load_dwordx4 %7, %9, off offset:112\n\tglobal_load_dwordx4 %8, %9, off offset:128"
               : "=&v"(d[0]), "=&v"(d[1]), "=&v"(d[2]), "=&v"(d[3]), "=&v"(d[4]), "=&v"(d[5]), "=&v"(d[6]), "=&v"(d[7]), "=&v"(d[8])
               : "v"(src) : "memory");
}
DI void u_landed(u32x4 (&d)[9]) {
  asm volatile("" : "+v"(d[0]), "+v"(d[1]), "+v"(d[2]), "+v"(d[3]), "+v"(d[4]), "+v"(d[5]), "+v"(d[6]), "+v"(d[7]), "+v"(d[8]) :: "memory");
}
DI void u_stage(unsigned ldsdst, const u16* __restrict__ w, int conv, const Tile& T, int uidx, int of, int lane) {
  const __amdgpu_buffer_rsrc_t rw = __builtin_amdgcn_make_buffer_rsrc((void*)w, (short)0, 512 * 512 * 9 * 2, 0x00020000);
#pragma unroll
  for (int k = 0; k < 9; ++k) {
    const int t = k * 64 + lane, row = t / 36, c16 = t - row * 36;
    dma1(ldsdst + k * 1024, (unsigned)(((T.nt * 64 + u_row(conv, of, row)) * 512 + 32 * (uidx >> 1)) * 18 + c16 * 16), rw, 0u);
  }
}
DI void u_touch(unsigned ldsb, const u16* __restrict__ w, int conv, const Tile& T, int uidx, int of, int lane) {
  const __amdgpu_buffer_rsrc_t rw = __builtin_amdgcn_make_buffer_rsrc((void*)w, (short)0, 512 * 512 * 9 * 2, 0x00020000);
#pragma unroll
  for (int h = 0; h < 2; ++h) {
    const int t = min(64 * h + lane, 79), row = t / 5;
    const unsigned vo = (unsigned)(((T.nt * 64 + u_row(conv, of, row)) * 512 + 32 * (uidx >> 1)) * 18 + (t - row * 5) * 128);
    asm volatile("s_mov_b32 m0, %0\n\ts_nop 0\n\tbuffer_load_dword %1, %2, 0 offen lds" :: "s"(ldsb + M_DUMP), "v"(vo), "s"(rw) : "memory");
  }
}
DI void u_fetch(u32x4 (&d)[9], const char* stage, int lane) {
#pragma unroll
  for (int k = 0; k < 9; ++k) d[k] = *(const u32x4*)(stage + (lane & 15) * 576 + (lane >> 4) * 144 + k * 16);
}
DI void u_emit(const u32x4 (&d)[9], char* ws, int conv, const Tile& T, int uidx, int of, int lane) {
  const __amdgpu_buffer_rsrc_t ru = __builtin_amdgcn_make_buffer_rsrc((void*)(ws + (conv == 0 ? OFF_U1 : OFF_U2)), (short)0, (int)U_BYTES, 0x00020000);
  const int dst = T.nt * 786432 + uidx * USLOT + of * 1024 + lane * 16;
  const float sg = (uidx & 1) ? 1.f : -1.f;
#pragma unroll
  for (int dy = 0; dy < 3; ++dy) {
    float ua[8], ub[8];
#pragma unroll
    for (int j = 0; j < 8; ++j) {
      float g[3];
#pragma unroll
      for (int dx = 0; dx < 3; ++dx) {
        const int e = 9 * j + 3 * dy + dx;
        const unsigned dw = d[e >> 3][(e >> 1) & 3];
        g[dx] = (e & 1) ? bf_hi(dw) : bf_lo(dw);
      }
      const float mid = 0.5f * (g[0] + sg * g[1] + g[2]);
      ua[j] = (uidx & 1) ? mid : g[0];
      ub[j] = (uidx & 1) ? g[2] : mid;
    }
    const u32x4 oa = {pack_bf16(ua[0], ua[1]), pack_bf16(ua[2], ua[3]), pack_bf16(ua[4], ua[5]), pack_bf16(ua[6], ua[7])};
    const u32x4 ob = {pack_bf16(ub[0], ub[1]), pack_bf16(ub[2], ub[3]), pack_bf16(ub[4], ub[5]), pack_bf16(ub[6], ub[7])};
    __builtin_amdgcn_raw_buffer_store_b128(oa, ru, dst + dy * 4096, 0, 16);
    __builtin_amdgcn_raw_buffer_store_b128(ob, ru, dst + 12288 + dy * 4096, 0, 16);
  }
}

template <int P> DI void epi1_mfma(const Comp& s, char* lds, char* ws, const Tile& T, unsigned ep, int wid, int lane) {
  const int i16 = lane & 15, kg = lane >> 4, ho = wid >> 1;
  f32x4 ye[4][2], yo[4][2];
  exchange<P>(s, lds, wid, lane, ye, yo);
  {
    const f32x4 e0 = *(const f32x4*)(lds + M_EMB + (32 * ho + 8 * kg) * 4), e1 = *(const f32x4*)(lds + M_EMB + (32 * ho + 8 * kg) * 4 + 16);
#pragma unroll
    for (int r = 0; r < 4; ++r) { ye[r][0] += e0; ye[r][1] += e1; yo[r][0] += e0; yo[r][1] += e1; }
  }
  const __amdgpu_buffer_rsrc_t rh = __builtin_amdgcn_make_buffer_rsrc((void*)(ws + OFF_HALO), (short)0, 256 * HALO_STRIDE, 0x00020000);
  float* red = (float*)(lds + M_RED);
  const int hb = T.bid * HALO_STRIDE;
  constexpr int RE = P == 0 ? 0 : 3;
#pragma unroll
  for (int f = 0; f < 2; ++f) {
    __builtin_amdgcn_raw_buffer_store_b128(__builtin_bit_cast(u32x4, ye[RE][f]), rh, hb + P * 8192 + ((ho * 2 + f) * 2) * 1024 + lane * 16, 0, 16);
    __builtin_amdgcn_raw_buffer_store_b128(__builtin_bit_cast(u32x4, yo[RE][f]), rh, hb + P * 8192 + ((ho * 2 + f) * 2 + 1) * 1024 + lane * 16, 0, 16);
  }
  if (i16 == (T.tx == 0 ? 15 : 0)) {
#pragma unroll
    for (int r = 0; r < 4; ++r)
#pragma unroll
      for (int f = 0; f < 2; ++f)
        __builtin_amdgcn_raw_buffer_store_b128(__builtin_bit_cast(u32x4, T.tx == 0 ? yo[r][f] : ye[r][f]), rh,
                                               hb + 16384 + (4 * P + r) * 256 + (32 * ho + 8 * kg + 4 * f) * 4, 0, 16);
  }
  {
    const float piv = __builtin_bit_cast(float, __builtin_amdgcn_readfirstlane(__builtin_bit_cast(int, ye[0][0][0])));
    f32x4 sv = {0.f, 0.f, 0.f, 0.f}, qv = {0.f, 0.f, 0.f, 0.f};
#pragma unroll
    for (int r = 0; r < 4; ++r)
#pragma unroll
      for (int f = 0; f < 2; ++f) {
        const f32x4 d0 = ye[r][f] - piv, d1 = yo[r][f] - piv;
        sv += d0 + d1;
        qv = __builtin_elementwise_fma(d0, d0, qv);
        qv = __builtin_elementwise_fma(d1, d1, qv);
      }
    const float sum = wave_sum((sv[0] + sv[1]) + (sv[2] + sv[3])), q = wave_sum((qv[0] + qv[1]) + (qv[2] + qv[3]));
    if (lane == 0) { red[wid] = sum + piv * 4096.f; red[8 + wid] = q - sum * sum * (1.f / 4096.f); }
  }
  lds_bar();
  wait_vm<0>();
  set_tag(ws, T.bid, W_HALO + wid, ep, lane);
  const int chl = lane & 31, half = lane >> 5;
  const float gam_c = ((const float*)(lds + M_GAM))[32 * ho + chl], bet_c = ((const float*)(lds + M_BET))[32 * ho + chl];
  lds_bar();
  Affine af;
  load_affine(af, lds, ho, kg);
  float* col = (float*)(lds + M_COL + wid * 512);
  {
    const float* raw = (const float*)(lds + M_RAW);
    const float sa = red[17] * gam_c, sb = bet_c - red[16] * sa;
    col[lane] = silu_f(raw[(4 * P + 1 + half) * 64 + 32 * ho + chl] * sa + sb);
    col[64 + lane] = silu_f(raw[(4 * P + 3 + half) * 64 + 32 * ho + chl] * sa + sb);
  }
  const __amdgpu_buffer_rsrc_t rv = __builtin_amdgcn_make_buffer_rsrc((void*)(ws + OFF_V2), (short)0, (int)V_BYTES, 0x00020000);
#pragma unroll
  for (int k = 0; k < 4; ++k) {
    f32x4 hl[2], hr[2];
    halo_ops(lds, col + k * 32, T.tx, kg, hl, hr);
    u32x4 o[4];
    row_transform(ye[k], yo[k], hl, hr, af, o);
    put_row(lds, rv, T, ho, 4 * P + k + 1, true, i16, kg, o);
    __builtin_amdgcn_sched_barrier(0);
  }
}

DI void epi1_loader(const Loader& L, const u16* __restrict__ w2, int uprod, int uidx, char* lds, char* ws, const Tile& T, unsigned ep, int wid, int lane) {
  const int i16 = lane & 15, kg = lane >> 4, p = wid & 1, ho = (wid >> 1) & 1;
  const __amdgpu_buffer_rsrc_t rh = __builtin_amdgcn_make_buffer_rsrc((void*)(ws + OFF_HALO), (short)0, 256 * HALO_STRIDE, 0x00020000);
  float* red = (float*)(lds + M_RED);
  {
    u32x4 dw2[9];
    u_load(dw2, w2, 1, T, uidx, wid - 4, lane);
    lds_bar();
    bar();
    wait_vm<0>();
    u_landed(dw2);
    u_emit(dw2, ws, 1, T, uidx, wid - 4, lane);
  }
  bar();
  float tot = 0.f, m2 = 0.f;
  if (wid == 4) {
    block_stats(red, tot, m2);
    group_stats(ws, 1, T, tot, m2, ep, lane);
  }
  wait_vm<0>();
  set_tag(ws, T.bid, W_U2 + (wid - 4), ep, lane);
  if (wid == 4) {
    group_stats_wait(ws, 1, T, tot, m2, ep, red, lane);
  }
  const int tyn = p == 0 ? T.ty - 1 : T.ty + 1;
  const bool have_v = tyn >= 0 && tyn < 8;
  const int bid_v = tile_bid(T.b, have_v ? tyn : T.ty, T.tx, T.nt);
  f32x4 ve[2], vo[2];
  const int hbv = bid_v * HALO_STRIDE + (p == 0 ? 8192 : 0) + ho * 4096 + lane * 16;
  if (wid != 4) {
    wait_tags<1>(ws, bid_v, W_HALO, have_v, ep);
#pragma unroll
    for (int f = 0; f < 2; ++f) {
      ve[f] = __builtin_bit_cast(f32x4, __builtin_amdgcn_raw_buffer_load_b128(rh, hbv + f * 2048, 0, 16));
      vo[f] = __builtin_bit_cast(f32x4, __builtin_amdgcn_raw_buffer_load_b128(rh, hbv + f * 2048 + 1024, 0, 16));
    }
  }
  if (wid == 5) {
    const int nb = T.bid ^ 32;
    wait_tags<1>(ws, nb, W_HALO, true, ep);
    const u32x4 c0 = __builtin_amdgcn_raw_buffer_load_b128(rh, nb * HALO_STRIDE + 16384 + lane * 16, 0, 16);
    const u32x4 c1 = __builtin_amdgcn_raw_buffer_load_b128(rh, nb * HALO_STRIDE + 16384 + 1024 + lane * 16, 0, 16);
    *(u32x4*)(lds + M_RAW + 256 + lane * 16) = c0;
    *(u32x4*)(lds + M_RAW + 256 + 1024 + lane * 16) = c1;
  } else if (wid >= 6) {
    const int tyd = wid == 6 ? T.ty - 1 : T.ty + 1;
    float cv = 0.f;
    if (tyd >= 0 && tyd < 8) {
      const int nb = tile_bid(T.b, tyd, T.tx ^ 1, T.nt);
      wait_tags<1>(ws, nb, W_HALO, true, ep);
      cv = __uint_as_float(__builtin_amdgcn_raw_buffer_load_b32(rh, nb * HALO_STRIDE + 16384 + (wid == 6 ? 7 : 0) * 256 + lane * 4, 0, 16));
    }
    ((float*)(lds + M_RAW))[(wid == 6 ? 0 : 9) * 64 + lane] = cv;
  }
  const int chl = lane & 31;
  const float gam_c = ((const float*)(lds + M_GAM))[32 * ho + chl], bet_c = ((const float*)(lds + M_BET))[32 * ho + chl];
  lds_bar();
  if (wid == 4) {
    wait_tags<1>(ws, bid_v, W_HALO, have_v, ep);
#pragma unroll
    for (int f = 0; f < 2; ++f) {
      ve[f] = __builtin_bit_cast(f32x4, __builtin_amdgcn_raw_buffer_load_b128(rh, hbv + f * 2048, 0, 16));
      vo[f] = __builtin_bit_cast(f32x4, __builtin_amdgcn_raw_buffer_load_b128(rh, hbv + f * 2048 + 1024, 0, 16));
    }
  }
  Affine af;
  load_affine(af, lds, ho, kg);
  float* col = (float*)(lds + M_COL + wid * 512);
  {
    const float* raw = (const float*)(lds + M_RAW);
    const float sa = red[17] * gam_c, sb = bet_c - red[16] * sa;
    col[lane] = silu_f(raw[9 * p * 64 + 32 * ho + chl] * sa + sb);
  }
  if (wid == 5) {
    wait_tags<1>(ws, uprod, W_U2, lane < 32 && lane != uidx, ep);
    lds_flag_set(lds, 3, ep);
  } else {
    lds_flag_wait(lds, 3, ep);
  }
  asm volatile("" : "+v"(ve[0]), "+v"(ve[1]), "+v"(vo[0]), "+v"(vo[1]));
  ld_u(L, 0); ld_u(L, 1); ld_u(L, 2);
  const __amdgpu_buffer_rsrc_t rv = __builtin_amdgcn_make_buffer_rsrc((void*)(ws + OFF_V2), (short)0, (int)V_BYTES, 0x00020000);
  f32x4 hl[2], hr[2];
  u32x4 o[4];
  halo_ops(lds, col, T.tx, kg, hl, hr);
  row_transform(ve, vo, hl, hr, af, o);
  if (!have_v) { o[0] = o[1] = o[2] = o[3] = u32x4{0u, 0u, 0u, 0u}; }
  put_row(lds, rv, T, ho, 9 * p, false, i16, kg, o);
  wait_vm<0>();
}

constexpr int xpiece(int k) { return k < 24 ? LDS_U + 2 * USLOT + k * 1024 : LDS_U + (k - 24) * 1024; }

template <int P> DI void epi2_mfma(const Comp& s, char* lds, char* ws, const Tile& T, u16* __restrict__ out, int wid, int lane) {
  const int i16 = lane & 15, kg = lane >> 4, ho = wid >> 1;
  bar();
  f32x4 ye[4][2], yo[4][2];
  exchange<P>(s, lds, wid, lane, ye, yo);
  const size_t off0 = ((size_t)(T.b * NCH + T.nt * 64 + 32 * ho + i16)) * HW + (T.y0 + 4 * P) * 64 + 32 * T.tx + 8 * kg;
  const __amdgpu_buffer_rsrc_t ro = __builtin_amdgcn_make_buffer_rsrc((void*)out, (short)0, 2 * NCH * HW * 2, 0x00020000);
#pragma unroll
  for (int f = 0; f < 2; ++f)
#pragma unroll
    for (int r = 0; r < 4; ++r) {
      const int k = (ho * 2 + f) * 8 + 4 * P + r;
      const u32x4 xv = *(const u32x4*)(lds + (k < 24 ? LDS_U + 2 * USLOT + k * 1024 : LDS_U + (k - 24) * 1024) + lane * 16);
      u32x4 o;
#pragma unroll
      for (int j = 0; j < 4; ++j) o[j] = pack_bf16(ye[r][f][j] + bf_lo(xv[j]), yo[r][f][j] + bf_hi(xv[j]));
      __builtin_amdgcn_raw_buffer_store_b128(o, ro, (int)((off0 + (size_t)f * 16 * HW + r * 64) * 2), 0, 16);
    }
}

template <int MODE> DI void loader_loop(const Loader& L, int uidx, int uprod, u32x4 (&ep8)[8], char* lds, char* ws, const Tile& T, unsigned ep, int w, int lane) {
  const int pty = T.ty - 1 + (lane >> 3);
  const bool need = lane < 24 && pty >= 0 && pty < 8 && (lane & 7) != T.nt;
  const int pbid = need ? tile_bid(T.b, pty, T.tx, lane & 7) : T.bid;
  constexpr int WV = MODE == 1 ? W_V1 : W_V2;
  lds_bar();
  u32x4 tg;
  {
    const char* tp = ws + OFF_LINES + (w == 0 ? pbid : T.bid) * 128 + WV * 4;
    asm volatile("global_load_dwordx4 %0, %1, off sc1" : "=&v"(tg) : "v"(tp) : "memory");
  }
  bar();
  ld_u(L, 3);
  if constexpr (MODE == 1) {
    if (w == 1) { if (!lds_flag_test(lds, 12, ep)) { wait_tags<1>(ws, uprod, W_U1, lane < 32 && lane != uidx, ep); lds_flag_set(lds, 12, ep); } }
    else lds_flag_wait(lds, 12, ep);
  }
  if (MODE == 1 && w == 3) {
    const __amdgpu_buffer_rsrc_t re = __builtin_amdgcn_make_buffer_rsrc((void*)(ws + OFF_EMBP), (short)0, 8 * 32 * 512, 0x00020000);
#pragma unroll
    for (int i = 0; i < 8; ++i) ep8[i] = __builtin_amdgcn_raw_buffer_load_b128(re, (T.nt * 32 + (lane >> 4) * 8 + i) * 512 + T.b * 256 + (lane & 15) * 16, 0, 16);
  }
  bar();
  ld_u(L, 4);
  wait_vm<12>();
  asm volatile("" : "+v"(tg) :: "memory");
  const bool early = w == 0 && __all(!need || (tg[0] == ep && tg[1] == ep && tg[2] == ep && tg[3] == ep));
  if (early) lds_flag_set(lds, MODE, ep);
  wait_vm<6>();
  bar();
  if (w == 0) {
    if (!early) { wait_tags<1>(ws, pbid, WV, need, ep); lds_flag_set(lds, MODE, ep); }
  } else {
    lds_flag_wait(lds, MODE, ep);
  }
  ld_v(L, 4); ld_v(L, 5); ld_u(L, 5);
  wait_vm<11>();
  bar();
#pragma unroll 1
  for (int t = 4; t < NUNIT - 2; ++t) {
    ld_v(L, t + 2); ld_u(L, t + 2);
    wait_vm<11>();
    bar();
  }
}

template <int MODE> DI void mfma_loop(Comp& s, char* ws, const u16* __restrict__ w2, const Tile& T, unsigned ldsb, unsigned ep, int wid, int lane) {
  const int bid = T.bid;
  lds_bar();
  lds_rd<0>(s.B[0][0], s.bbase); lds_rd<1024>(s.B[0][1], s.bbase); lds_rd<0>(s.A[0], s.abase);
  lds_rd<4096>(s.B[0][2], s.bbase); lds_rd<5120>(s.B[0][3], s.bbase); lds_rd<8192>(s.B[0][4], s.bbase); lds_rd<9216>(s.B[0][5], s.bbase);
  lds_rd<1024>(s.A[1], s.abase); lds_rd<2048>(s.A[2], s.abase); lds_rd<3072>(s.A[3], s.abase);
  wait_lgkm<3>();
  s.anxt = s.abase;
  unsigned voff = 0, uoff = 0;
  unit<MODE, 0, true>(s, voff, uoff);
  wait_vm<0>();
  set_tag(ws, bid, (MODE == 1 ? W_V1 : W_V2) + wid, ep, fresh_lane());
  if (MODE == 1) u_touch(ldsb, w2, 1, T, (T.b * 8 + T.ty) * 2 + T.tx, wid, fresh_lane());
  unit<MODE, 1, true>(s, voff, uoff);
#pragma unroll 1
  for (int c = 1; c < 16; ++c) {
    unit<MODE, 0>(s, voff, uoff);
    unit<MODE, 1>(s, voff, uoff);
  }
  asm volatile("s_waitcnt lgkmcnt(0)\n\ts_nop 15" ::: "memory");
}
__global__ __launch_bounds__(512, 2) void k_main(const u16* __restrict__ x, const u16* __restrict__ temb, const u16* __restrict__ g1, const u16* __restrict__ be1,
                                                 const u16* __restrict__ w1, const u16* __restrict__ wemb, const u16* __restrict__ g2,
                                                 const u16* __restrict__ be2, const u16* __restrict__ w2, u16* __restrict__ out, char* __restrict__ ws) {
  __shared__ __attribute__((aligned(16))) char lds[LDS_TOTAL];
  const int tid = threadIdx.x, lane = tid & 63;
  const int wid = __builtin_amdgcn_readfirstlane(tid >> 6);
  const int bid = blockIdx.x, xcd = bid & 7, slot = bid >> 3;
  Tile T;
  T.bid = bid; T.b = xcd >> 2; T.nt = (xcd & 1) * 4 + (slot & 3); T.ty = ((xcd >> 1) & 1) * 4 + (slot >> 3); T.tx = (slot >> 2) & 1; T.y0 = T.ty * 8;
  const unsigned ldsb = (unsigned)(size_t)lds;
  const int p = wid & 1, ho = (wid >> 1) & 1;
  rows_stage(wid < 4 ? 4 * p + 1 : 9 * p, wid < 4 ? 4 : 1, ldsb, x, T, ho, lane);
  {
    const __amdgpu_buffer_rsrc_t rc = __builtin_amdgcn_make_buffer_rsrc((void*)__builtin_amdgcn_s_getpc(), (short)0, CODE_BYTES, 0x00020000);
    asm volatile("s_mov_b32 m0, %0\n\ts_nop 0\n\tbuffer_load_dword %1, %2, 0 offen lds" :: "s"(ldsb + M_DUMP), "v"((unsigned)tid * 128u), "s"(rc) : "memory");
  }
  unsigned epv = __hip_atomic_load((gu32*)(ws + OFF_EPOCH), RLX_AGENT);
  float* red = (float*)(lds + M_RED);

  if (wid >= 4) {
    const int w = wid - 4;
    const int uidx = (T.b * 8 + T.ty) * 2 + T.tx;
    const int rho[1] = {9 * p};
    const char* stage = lds + LDS_U + w * 9216;
    u_stage(ldsb + LDS_U + w * 9216, w1, 0, T, uidx, w, lane);
    XRows<1> X;
    rows_params<1>(X, g1, be1, T, ho, lane);
    u32x4 we[3];
    unsigned short te[2][3];
    if (w == 3) {
#pragma unroll
      for (int i = 0; i < 3; ++i) {
        const int k = 24 * uidx + 3 * (lane >> 3) + i;
        we[i] = *(const u32x4*)(wemb + (size_t)k * 512 + T.nt * 64 + 8 * (lane & 7));
        te[0][i] = temb[k]; te[1][i] = temb[NEMB + k];
      }
    }
    wait_vm<0>();
    asm volatile("" : "+v"(epv));
    const unsigned ep = __builtin_amdgcn_readfirstlane(epv) + 1u;
    rows_fetch<1>(X, rho, 9 * p, 9 * p, lds, T, ho, lane);
    {
      u32x4 dw1[9];
      u_fetch(dw1, stage, lane);
      u_emit(dw1, ws, 0, T, uidx, w, lane);
    }
    if (w == 3) {
      float a[2][8];
#pragma unroll
      for (int j = 0; j < 8; ++j) a[0][j] = a[1][j] = 0.f;
#pragma unroll
      for (int i = 0; i < 3; ++i) {
        const float t0 = silu_f(__uint_as_float((unsigned)te[0][i] << 16)), t1 = silu_f(__uint_as_float((unsigned)te[1][i] << 16));
#pragma unroll
        for (int j = 0; j < 8; ++j) {
          const float wv = (j & 1) ? bf_hi(we[i][j >> 1]) : bf_lo(we[i][j >> 1]);
          a[0][j] += t0 * wv; a[1][j] += t1 * wv;
        }
      }
      float* sc = (float*)(lds + M_COL);
#pragma unroll
      for (int j = 0; j < 8; ++j) { sc[(lane >> 3) * 128 + (lane & 7) * 8 + j] = a[0][j]; sc[(lane >> 3) * 128 + 64 + (lane & 7) * 8 + j] = a[1][j]; }
      float e0 = 0.f, e1 = 0.f;
#pragma unroll
      for (int g = 0; g < 8; ++g) { e0 += sc[g * 128 + lane]; e1 += sc[g * 128 + 64 + lane]; }
      const __amdgpu_buffer_rsrc_t re = __builtin_amdgcn_make_buffer_rsrc((void*)(ws + OFF_EMBP), (short)0, 8 * 32 * 512, 0x00020000);
      __builtin_amdgcn_raw_buffer_store_b32(__float_as_uint(e0), re, (T.nt * 32 + uidx) * 512 + lane * 4, 0, 16);
      __builtin_amdgcn_raw_buffer_store_b32(__float_as_uint(e1), re, (T.nt * 32 + uidx) * 512 + 256 + lane * 4, 0, 16);
    }
    wait_vm<0>();
    set_tag(ws, T.bid, W_U1 + w, ep, lane);
    lds_bar();
    Loader L;
    L.nt = T.nt;
    L.rv = __builtin_amdgcn_make_buffer_rsrc((void*)(ws + OFF_V1), (short)0, (int)V_BYTES, 0x00020000);
    L.ru = __builtin_amdgcn_make_buffer_rsrc((void*)(ws + OFF_U1), (short)0, (int)U_BYTES, 0x00020000);
    L.vmid = lane * 16;
    L.vfirst = (T.y0 == 0 && !(w & 1)) ? 0x80000000u : L.vmid;
    L.vlast = (T.y0 == 56 && (w & 1)) ? 0x80000000u : L.vmid;
    L.sv = (unsigned)T.b * 16u * 524288u + (unsigned)(w >> 1) * 131072u + (unsigned)((T.y0 + 5 * (w & 1)) * 2048 + T.tx * 1024) - 2048u;
    L.su = (unsigned)T.nt * 786432u + (unsigned)w * 6144u;
    L.mv = ldsb + (w >> 1) * 10240 + (w & 1) * 5120;
    L.mu = ldsb + LDS_U + w * 6144;
    const int uprod = tile_bid((lane >> 4) & 1, (lane >> 1) & 7, lane & 1, T.nt);
    u32x4 tg;
    {
      const char* tp = ws + OFF_LINES + (w == 1 ? uprod : T.bid) * 128 + W_U1 * 4;
      asm volatile("global_load_dwordx4 %0, %1, off sc1" : "=&v"(tg) : "v"(tp) : "memory");
    }
    rows_column<1>(X, lds, wid, lane);
    rows_emit<1>(X, rho, 0, 1, lds, ws, T, wid, ho, lane);
    wait_vm<0>();
    asm volatile("" : "+v"(tg) :: "memory");
    if (w == 1) {
      const bool ok = tg[0] == ep && tg[1] == ep && tg[2] == ep && tg[3] == ep;
      const bool need = lane < 32 && lane != uidx, own = need && (lane >> 2) == T.nt;
      if (!__all(!own || ok)) wait_tags<1>(ws, uprod, W_U1, own, ep);
      if (__all(!need || ok)) lds_flag_set(lds, 12, ep);
      lds_flag_set(lds, 0, ep);
    } else {
      lds_flag_wait(lds, 0, ep);
    }
    ld_u(L, 0); ld_u(L, 1); ld_u(L, 2);
    wait_vm<0>();
    u32x4 ep8[8];
    loader_loop<1>(L, uidx, uprod, ep8, lds, ws, T, ep, w, lane);
    if (w == 3) {
      f32x4 e = {0.f, 0.f, 0.f, 0.f};
#pragma unroll
      for (int i = 0; i < 8; ++i) e += __builtin_bit_cast(f32x4, ep8[i]);
#pragma unroll
      for (int j = 0; j < 4; ++j) { e[j] += __shfl_xor(e[j], 16); e[j] += __shfl_xor(e[j], 32); }
      if (lane < 16) *(f32x4*)(lds + M_EMB + lane * 16) = e;
    }
    wait_vm<0>();
    bar();
    bar();
    L.rv = __builtin_amdgcn_make_buffer_rsrc((void*)(ws + OFF_V2), (short)0, (int)V_BYTES, 0x00020000);
    L.ru = __builtin_amdgcn_make_buffer_rsrc((void*)(ws + OFF_U2), (short)0, (int)U_BYTES, 0x00020000);
    epi1_loader(L, w2, uprod, uidx, lds, ws, T, ep, wid, lane);
    loader_loop<2>(L, uidx, uprod, ep8, lds, ws, T, ep, w, lane);
    {
      const __amdgpu_buffer_rsrc_t rx = __builtin_amdgcn_make_buffer_rsrc((void*)x, (short)0, 2 * NCH * HW * 2, 0x00020000);
      const unsigned vx = (lane & 15) * (HW * 2) + (lane >> 4) * 16;
      const unsigned sx = (unsigned)((T.b * NCH + T.nt * 64) * HW + T.y0 * 64 + 32 * T.tx) * 2u;
#pragma unroll
      for (int q = 0; q < 6; ++q) { const int k = w * 6 + q; dma1(ldsb + xpiece(0) + k * 1024, vx, rx, sx + (unsigned)(k >> 3) * 16u * HW * 2u + (unsigned)(k & 7) * 128u); }
      wait_vm<6>();
      bar();
#pragma unroll
      for (int q = 0; q < 2; ++q) { const int k = 24 + w * 2 + q; dma1(ldsb + xpiece(24) + (k - 24) * 1024, vx, rx, sx + (unsigned)(k >> 3) * 16u * HW * 2u + (unsigned)(k & 7) * 128u); }
      bar();
      wait_vm<0>();
      bar();
    }
    return;
  }

  const int i16 = lane & 15, kg = lane >> 4;
  unsigned ep;
  {
    const int rho[4] = {4 * p + 1, 4 * p + 2, 4 * p + 3, 4 * p + 4};
    XRows<4> X;
    rows_params<4>(X, g1, be1, T, ho, lane);
    unsigned short g2c = 0, b2c = 0;
    if (wid == 1) { g2c = g2[T.nt * 64 + lane]; b2c = be2[T.nt * 64 + lane]; }
    wait_vm<0>();
    asm volatile("" : "+v"(epv));
    ep = __builtin_amdgcn_readfirstlane(epv) + 1u;
    rows_fetch<4>(X, rho, 4 * p + 1 + (lane >> 5), 4 * p + 3 + (lane >> 5), lds, T, ho, lane);
    rows_stats<4, 4>(X, red, wid, lane);
    if (wid == 1) {
      if (lane < 8) ((float*)(lds + M_ZERO))[lane] = 0.f;
      ((float*)(lds + M_GAM))[lane] = __uint_as_float((unsigned)g2c << 16);
      ((float*)(lds + M_BET))[lane] = __uint_as_float((unsigned)b2c << 16);
    }
    asm volatile("s_waitcnt lgkmcnt(0)" ::: "memory");
    lds_flag_set(lds, 4 + wid, ep);
    if (wid == 0) {
#pragma unroll
      for (int k = 1; k < 4; ++k) lds_flag_wait(lds, 4 + k, ep);
      float tot, m2;
      block_stats(red, tot, m2);
      group_stats(ws, 0, T, tot, m2, ep, lane);
      group_stats_wait(ws, 0, T, tot, m2, ep, red, lane);
    }
    lds_bar();
    rows_column<4>(X, lds, wid, lane);
    rows_emit<4>(X, rho, 0, 4, lds, ws, T, wid, ho, lane);
  }
  Comp s;
  s.abase = ldsb + p * 10240 + i16 * 64 + vpos(kg, i16) * 16;
  s.bbase = ldsb + LDS_U + p * 12288 + ho * 2048 + lane * 16;
  mfma_loop<1>(s, ws, w2, T, ldsb, ep, wid, lane);
  bar();
  if (p == 0) epi1_mfma<0>(s, lds, ws, T, ep, wid, fresh_lane()); else epi1_mfma<1>(s, lds, ws, T, ep, wid, fresh_lane());
  mfma_loop<2>(s, ws, w2, T, ldsb, ep, wid, lane);
  if (p == 0) epi2_mfma<0>(s, lds, ws, T, out, wid, fresh_lane()); else epi2_mfma<1>(s, lds, ws, T, out, wid, fresh_lane());
  const int le = fresh_lane();
  if (wid == 0 && le < 16) ((unsigned*)(lds + M_RED + 96))[le] = 0u;
  if (bid == 0 && wid == 0 && le == 0) __hip_atomic_store((gu32*)(ws + OFF_EPOCH), ep, RLX_AGENT);
}

extern "C" void kernel_launch(void* const* d_in, const int* in_sizes, int n_in, void* d_out, int out_size, void* d_ws, size_t ws_size,
                              hipStream_t stream) {
  k_main<<<256, 512, 0, stream>>>((const u16*)d_in[0], (const u16*)d_in[1], (const u16*)d_in[2], (const u16*)d_in[3], (const u16*)d_in[4],
                                  (const u16*)d_in[5], (const u16*)d_in[6], (const u16*)d_in[7], (const u16*)d_in[8], (u16*)d_out, (char*)d_ws);
}
```
